```python
import torch

def _softmax(x, axis=-1):
    return torch.softmax(x, dim=axis)
def _gelu(x):
    return torch.nn.functional.gelu(x, approximate="tanh")
def _silu(x):
    return torch.nn.functional.silu(x)
def _sigmoid(x):
    return torch.sigmoid(x)
def _layernorm(x, axis=-1, eps=1e-5):
    m = x.mean(dim=axis, keepdim=True)
    v = x.var(dim=axis, keepdim=True, unbiased=False)
    return (x - m) / torch.sqrt(v + eps)
def _scatter_add(tgt, idx, src, axis=0):
    return tgt.clone().index_add_(axis, idx, src)
def _segment_sum(src, seg, n):
    out = torch.zeros((n,) + src.shape[1:], dtype=src.dtype)
    return out.index_add_(0, seg, src)
def _pad(x, pw):
    flat = tuple(v for pair in reversed(pw) for v in pair)
    return torch.nn.functional.pad(x, flat)

kernel_name = 'standalone__spectral_fnet__prompt'

def setup_inputs():

    _w = (lambda a: (lambda t: t.to(torch.bfloat16) if t.is_floating_point() else t)(torch.as_tensor(a)))
    B, S, D = 1, 2048, 640
    ns = torch.arange(S)[:, None] * torch.arange(S)[None, :]
    nd = torch.arange(D)[:, None] * torch.arange(D)[None, :]
    Ws_re = torch.cos(-2.0 * torch.pi * ns / S).to(float)
    Ws_im = torch.sin(-2.0 * torch.pi * ns / S).to(float)
    Wd_re = torch.cos(-2.0 * torch.pi * nd / D).to(float)
    Wd_im = torch.sin(-2.0 * torch.pi * nd / D).to(float)
    return {
        "x": _w((lambda *s: torch.randn(*s, dtype=torch.bfloat16) * 0.05)(B, S, D)),
        "dft_seq_re": _w(Ws_re),
        "dft_seq_im": _w(Ws_im),
        "dft_hid_re": _w(Wd_re),
        "dft_hid_im": _w(Wd_im),
    }

def reference(x, dft_seq_re, dft_seq_im, dft_hid_re, dft_hid_im):
    a = torch.einsum("bsd,ed->bse", x, dft_hid_re)
    b = torch.einsum("bsd,ed->bse", x, dft_hid_im)
    y = torch.einsum("ts,bse->bte", dft_seq_re, a) - torch.einsum("ts,bse->bte", dft_seq_im, b)
    return y
```

```cpp
#include <hip/hip_runtime.h>
#include <hip/hip_bf16.h>
typedef __attribute__((ext_vector_type(8))) short bf16x8;
typedef __attribute__((ext_vector_type(4))) float f32x4;
typedef unsigned short u16;
static __device__ __forceinline__ u16 f2bf(float f){ __hip_bfloat16 h = __float2bfloat16(f); return *reinterpret_cast<u16*>(&h);}

__global__ __launch_bounds__(256) void k_stage1(const u16* __restrict__ Wre, const u16* __restrict__ Wim, const u16* __restrict__ x, u16* __restrict__ abT){
  int wid = threadIdx.x>>6, lane = threadIdx.x&63, fr = lane&15, fq = lane>>4;
  int m0 = blockIdx.y*64 + (wid>>1)*32, n0 = blockIdx.x*64 + (wid&1)*32;
  const u16* A = m0 < 640 ? Wre + (size_t)m0*640 : Wim + (size_t)(m0-640)*640;
  float sg = m0 < 640 ? 1.f : -1.f;
  const u16* B = x + (size_t)n0*640;
  f32x4 acc[2][2] = {};
  for (int k=0;k<640;k+=32){
    bf16x8 a[2], b[2];
    for(int i=0;i<2;i++){ a[i] = *(const bf16x8*)(A + (size_t)(i*16+fr)*640 + k + fq*8); b[i] = *(const bf16x8*)(B + (size_t)(i*16+fr)*640 + k + fq*8);}
    for(int i=0;i<2;i++)for(int j=0;j<2;j++) acc[i][j] = __builtin_amdgcn_mfma_f32_16x16x32_bf16(a[i],b[j],acc[i][j],0,0,0);
  }
  for(int i=0;i<2;i++)for(int j=0;j<2;j++)for(int r=0;r<4;r++)
    abT[(size_t)(m0+i*16+fq*4+r)*2048 + n0+j*16+fr] = f2bf(sg*acc[i][j][r]);
}
__global__ __launch_bounds__(256) void k_stage2(const u16* __restrict__ Sre, const u16* __restrict__ Sim, const u16* __restrict__ abT, u16* __restrict__ y){
  int wid = threadIdx.x>>6, lane = threadIdx.x&63, fr = lane&15, fq = lane>>4;
  int m0 = blockIdx.y*64 + (wid>>1)*32, n0 = blockIdx.x*64 + (wid&1)*32;
  f32x4 acc[2][2] = {};
  for (int h=0;h<2;h++){
    const u16* A = (h? Sim:Sre) + (size_t)m0*2048;
    const u16* B = abT + (size_t)(h*640+n0)*2048;
    #pragma unroll 4
    for (int k=0;k<2048;k+=32){
      bf16x8 a[2], b[2];
      for(int i=0;i<2;i++){ a[i] = *(const bf16x8*)(A + (size_t)(i*16+fr)*2048 + k + fq*8); b[i] = *(const bf16x8*)(B + (size_t)(i*16+fr)*2048 + k + fq*8);}
      for(int i=0;i<2;i++)for(int j=0;j<2;j++) acc[i][j] = __builtin_amdgcn_mfma_f32_16x16x32_bf16(a[i],b[j],acc[i][j],0,0,0);
    }
  }
  for(int i=0;i<2;i++)for(int j=0;j<2;j++)for(int r=0;r<4;r++)
    y[(size_t)(m0+i*16+fq*4+r)*640 + n0+j*16+fr] = f2bf(acc[i][j][r]);
}

extern "C" void kernel_launch(void* const* d_in, const int* in_sizes, int n_in,
                              void* d_out, int out_size, void* d_ws, size_t ws_size,
                              hipStream_t stream) {
  const u16* x = (const u16*)d_in[0];
  const u16* Sre = (const u16*)d_in[1];
  const u16* Sim = (const u16*)d_in[2];
  const u16* Wre = (const u16*)d_in[3];
  const u16* Wim = (const u16*)d_in[4];
  u16* abT = (u16*)d_ws;
  k_stage1<<<dim3(2048/64, 1280/64), 256, 0, stream>>>(Wre, Wim, x, abT);
  k_stage2<<<dim3(640/64, 2048/64), 256, 0, stream>>>(Sre, Sim, abT, (u16*)d_out);
}
```

```cpp
#include <hip/hip_runtime.h>
#include <hip/hip_bf16.h>

typedef __attribute__((ext_vector_type(8))) __bf16 bf16x8;
typedef __attribute__((ext_vector_type(2))) __bf16 bf16x2;
typedef __attribute__((ext_vector_type(4))) float f32x4;
typedef __attribute__((ext_vector_type(2))) float f32x2;
typedef __attribute__((ext_vector_type(4))) unsigned u32x4;
typedef __attribute__((ext_vector_type(2))) unsigned u32x2;
typedef unsigned long long u64;
#define DI __device__ __forceinline__
#define LAS __attribute__((address_space(3)))

#define ROT1 1
#define ROT2 1

constexpr int S_ = 2048, D_ = 640;
constexpr unsigned AB_BYTES = 4u * 4 * 16 * 20480;
constexpr unsigned XCH_OFF = AB_BYTES;
constexpr unsigned XCH_BYTES = 256u * 3 * 4 * 2560;
constexpr unsigned FLAG_OFF = XCH_OFF + XCH_BYTES;
constexpr unsigned FLAG_BYTES = 256u * 256;
constexpr unsigned STAMP_OFF = FLAG_OFF + FLAG_BYTES;

DI unsigned rfl(unsigned x) { return __builtin_amdgcn_readfirstlane(x); }
DI __amdgpu_buffer_rsrc_t mkrsrc(const void* p, unsigned bytes) { return __builtin_amdgcn_make_buffer_rsrc((void*)p, 0, bytes, 0x00020000); }
template <int OFF> DI void dma(unsigned voff, __amdgpu_buffer_rsrc_t srd, unsigned soff) {
  asm volatile("buffer_load_dwordx4 %0, %1, %2 offen offset:%3 lds" ::"v"(voff), "s"(srd), "s"(soff), "n"(OFF) : "memory");
}
DI void setm0(unsigned m) { asm volatile("s_mov_b32 m0, %0\n\ts_nop 0" ::"s"(m) : "memory"); }
template <int N> DI void waitv() { asm volatile("s_waitcnt vmcnt(%0)" ::"n"(N) : "memory"); }
DI void bar() { asm volatile("s_barrier" ::: "memory"); }
DI void waitl0_bar() { asm volatile("s_waitcnt lgkmcnt(0)\n\ts_barrier" ::: "memory"); }
DI bf16x8 ldsr(unsigned addr) { return *(const LAS bf16x8*)(size_t)addr; }
DI unsigned pk(float a, float b) { f32x2 v = {a, b}; bf16x2 h = __builtin_convertvector(v, bf16x2); return __builtin_bit_cast(unsigned, h); }
DI float lo16(unsigned u) { return __uint_as_float(u << 16); }
DI float hi16(unsigned u) { return __uint_as_float(u & 0xffff0000u); }
#define MFMA(a, b, c) __builtin_amdgcn_mfma_f32_16x16x32_bf16(a, b, c, 0, 0, 0)

#define ST(k)

constexpr int NS1 = 5, STG1 = 26 * 1024;
__global__ __launch_bounds__(512, 2) void k_stage1(const char* __restrict__ x, const char* __restrict__ wre, const char* __restrict__ wim, char* __restrict__ ws) {
  constexpr int KID = 0;
  ST(0);
  const int lane = threadIdx.x & 63, w = rfl(threadIdx.x >> 6);
  const int b = blockIdx.x, xcd = b & 7, i = b >> 3;
  const int q = xcd >> 1, part = q >> 1, sh = q & 1, th = xcd & 1, sb = i >> 3, es = i & 7;
  const int s0 = sh * 1024 + th * 512 + sb * 128;
  if (w >= 4) {
    const int lw = w - 4;
    const __amdgpu_buffer_rsrc_t sX = mkrsrc(x, S_ * D_ * 2), sW = mkrsrc(part ? wim : wre, D_ * D_ * 2);
    unsigned va[4], vb[3];
#pragma unroll
    for (int p = 0; p < 4; p++) { int r = (lw * 4 + p) * 8 + (lane >> 3); va[p] = (s0 + r) * 1280 + (((lane & 7) ^ ((r >> 1) & 7)) * 16) - p * 1024; }
    const int nb0 = lw < 2 ? lw * 3 : 6 + (lw - 2) * 2;
#pragma unroll
    for (int p = 0; p < 3; p++) { int r = (nb0 + p) * 8 + (lane >> 3); if (r > 79) r = 79; vb[p] = (es * 80 + r) * 1280 + (((lane & 7) ^ ((r >> 1) & 7)) * 16) - p * 1024; }
    int k = ROT1 ? (es + 2 * sb) % 10 : 0;
    unsigned slot = 0;
    auto issue = [&]() {
      const unsigned ko = k * 128;
      setm0(slot + lw * 4096);
      dma<0>(va[0], sX, ko); dma<1024>(va[1], sX, ko); dma<2048>(va[2], sX, ko); dma<3072>(va[3], sX, ko);
      setm0(slot + 16384 + nb0 * 1024);
      dma<0>(vb[0], sW, ko); dma<1024>(vb[1], sW, ko);
      if (lw < 2) dma<2048>(vb[2], sW, ko);
      k = k == 9 ? 0 : k + 1;
      slot = slot == (NS1 - 1) * STG1 ? 0 : slot + STG1;
    };
    if (lw == 0) ((unsigned*)(ws + FLAG_OFF))[b * 64 + lane] = 0;
    for (int s = 0; s < NS1; s++) issue();
    if (lw < 2) waitv<7 * (NS1 - 1)>(); else waitv<6 * (NS1 - 1)>();
    bar();
#pragma unroll 1
    for (int s = 0; s < 10 - NS1; s++) {
      if (lw < 2) waitv<7 * (NS1 - 2)>(); else waitv<6 * (NS1 - 2)>();
      bar();
      issue();
    }
    if (lw < 2) { waitv<7 * 3>(); bar(); waitv<7 * 2>(); bar(); waitv<7>(); bar(); waitv<0>(); bar(); }
    else        { waitv<6 * 3>(); bar(); waitv<6 * 2>(); bar(); waitv<6>(); bar(); waitv<0>(); bar(); }
    ST(1);
    return;
  }
  const int fr = lane & 15, g = lane >> 4;
  const unsigned sw = (unsigned)((g ^ (fr >> 1)) * 16);
  const unsigned a0 = (w * 32 + fr) * 128 + sw, b0 = 16384 + fr * 128 + sw;
  f32x4 acc[2][5] = {};
  bf16x8 A0[2], B0[5], A1[2], B1[5];
  unsigned slot = 0;
  asm volatile("" ::"s"(ws));
  bar();
  ST(1);
#pragma unroll
  for (int m = 0; m < 2; m++) A0[m] = ldsr(a0 + m * 2048);
#pragma unroll
  for (int n = 0; n < 5; n++) B0[n] = ldsr(b0 + n * 2048);
  auto half1 = [&]() {
    const unsigned a1 = (a0 ^ 64) + slot, b1 = (b0 ^ 64) + slot;
#pragma unroll
    for (int m = 0; m < 2; m++) A1[m] = ldsr(a1 + m * 2048);
#pragma unroll
    for (int n = 0; n < 5; n++) B1[n] = ldsr(b1 + n * 2048);
#pragma unroll
    for (int n = 0; n < 5; n++)
#pragma unroll
      for (int m = 0; m < 2; m++) acc[m][n] = MFMA(A0[m], B0[n], acc[m][n]);
#pragma unroll
    for (int u = 0; u < 7; u++) { __builtin_amdgcn_sched_group_barrier(0x8, 1, 0); __builtin_amdgcn_sched_group_barrier(0x100, 1, 0); }
    __builtin_amdgcn_sched_group_barrier(0x8, 3, 0);
  };
#pragma unroll 1
  for (int s = 0; s < 9; s++) {
    half1();
    slot = slot == (NS1 - 1) * STG1 ? 0 : slot + STG1;
    waitl0_bar();
    const unsigned an = a0 + slot, bn = b0 + slot;
#pragma unroll
    for (int m = 0; m < 2; m++) A0[m] = ldsr(an + m * 2048);
#pragma unroll
    for (int n = 0; n < 5; n++) B0[n] = ldsr(bn + n * 2048);
#pragma unroll
    for (int n = 0; n < 5; n++)
#pragma unroll
      for (int m = 0; m < 2; m++) acc[m][n] = MFMA(A1[m], B1[n], acc[m][n]);
#pragma unroll
    for (int u = 0; u < 7; u++) { __builtin_amdgcn_sched_group_barrier(0x8, 1, 1); __builtin_amdgcn_sched_group_barrier(0x100, 1, 1); }
    __builtin_amdgcn_sched_group_barrier(0x8, 3, 1);
  }
  half1();
#pragma unroll
  for (int n = 0; n < 5; n++)
#pragma unroll
    for (int m = 0; m < 2; m++) acc[m][n] = MFMA(A1[m], B1[n], acc[m][n]);
  ST(2);
  const float sg = part ? -1.f : 1.f;
  const int sq = th * 512 + sb * 128 + w * 32;
  char* dst = ws + (((q * 4 + (es >> 1)) * 16 + (sq >> 6)) * 10 + (es & 1) * 5) * 2048 + ((sq >> 5) & 1) * 1024 + ((2 * (g & 1) + (g >> 1)) * 16 + fr) * 16;
#pragma unroll
  for (int n = 0; n < 5; n++) {
    unsigned x0 = pk(sg * acc[0][n][0], sg * acc[0][n][1]), x1 = pk(sg * acc[0][n][2], sg * acc[0][n][3]);
    unsigned y0 = pk(sg * acc[1][n][0], sg * acc[1][n][1]), y1 = pk(sg * acc[1][n][2], sg * acc[1][n][3]);
    auto r0 = __builtin_amdgcn_permlane16_swap(x0, y0, false, false);
    auto r1 = __builtin_amdgcn_permlane16_swap(x1, y1, false, false);
    *(u32x4*)(dst + n * 2048) = (u32x4){r0[0], r1[0], r0[1], r1[1]};
  }
  ST(3);
}

constexpr int NS2 = 4, STG2 = 36 * 1024;
__global__ __launch_bounds__(512, 2) void k_stage2(const char* __restrict__ sre, const char* __restrict__ sim, char* __restrict__ ws, char* __restrict__ y) {
  constexpr int KID = 1;
  ST(0);
  const int lane = threadIdx.x & 63, w = rfl(threadIdx.x >> 6);
  const int b = blockIdx.x, xcd = b & 7, i = b >> 3;
  const int q = xcd >> 1, part = q >> 1, sh = q & 1, th = xcd & 1, tb = th * 8 + (i >> 2), et = i & 3;
  if (w >= 4) {
    const int lw = w - 4;
    const __amdgpu_buffer_rsrc_t sA = mkrsrc(part ? sim : sre, S_ * S_ * 2), sB = mkrsrc(ws, AB_BYTES);
    unsigned va[4];
#pragma unroll
    for (int p = 0; p < 4; p++) {
      int r = (lw * 4 + p) * 8 + (lane >> 3);
      int t = tb * 128 + (r & 64) + ((((r >> 4) + q) & 3) << 4) + (r & 15);
      va[p] = t * 4096 + sh * 2048 + (((lane & 7) ^ ((r >> 1) & 7)) * 16) - p * 1024;
    }
    const unsigned vb = lane * 16 + lw * 5120;
    const unsigned bbase = (q * 4 + et) * 16 * 20480;
    int k = ROT2 ? ((et + (i >> 2)) & 3) : 0, kb = 0;
    unsigned slot = 0;
    auto issue = [&]() {
      const int ks = kb + k;
      const unsigned ao = ks * 128, bo = bbase + ks * 20480;
      setm0(slot + lw * 4096);
      dma<0>(va[0], sA, ao); dma<1024>(va[1], sA, ao); dma<2048>(va[2], sA, ao); dma<3072>(va[3], sA, ao);
      setm0(slot + 16384 + lw * 5120);
      dma<0>(vb, sB, bo); dma<1024>(vb, sB, bo); dma<2048>(vb, sB, bo); dma<3072>(vb, sB, bo);
      setm0(slot + 16384 + lw * 5120 + 4096);
      dma<0>(vb, sB, bo + 4096);
      k = (k + 1) & 3;
      slot = slot == (NS2 - 1) * STG2 ? 0 : slot + STG2;
      if (slot == 0) kb += 4;
    };
    for (int s = 0; s < NS2; s++) issue();
    waitv<9 * 3>(); bar();
#pragma unroll 1
    for (int s = 0; s < 16 - NS2; s++) { waitv<9 * 2>(); bar(); issue(); }
    waitv<9 * 2>(); bar(); waitv<9>(); bar(); waitv<0>(); bar();
    ST(1);
    return;
  }
  const int fr = lane & 15, g = lane >> 4, wm = w >> 1, wn = w & 1;
  const unsigned a0 = (wm * 64 + fr) * 128 + (unsigned)((g ^ (fr >> 1)) * 16), b0 = 16384 + wn * 10240 + lane * 16;
  f32x4 acc[4][5] = {};
  bf16x8 A0[4], B0[5], A1[4], B1[5];
  unsigned slot = 0;
  asm volatile("" ::"s"(y), "s"(ws));
  bar();
  ST(1);
#pragma unroll
  for (int m = 0; m < 4; m++) A0[m] = ldsr(a0 + m * 2048);
#pragma unroll
  for (int n = 0; n < 5; n++) B0[n] = ldsr(b0 + n * 2048);
  auto half1 = [&]() {
    const unsigned a1 = (a0 ^ 64) + slot, b1 = b0 + 1024 + slot;
#pragma unroll
    for (int m = 0; m < 4; m++) A1[m] = ldsr(a1 + m * 2048);
#pragma unroll
    for (int n = 0; n < 5; n++) B1[n] = ldsr(b1 + n * 2048);
#pragma unroll
    for (int n = 0; n < 5; n++)
#pragma unroll
      for (int m = 0; m < 4; m++) acc[m][n] = MFMA(B0[n], A0[m], acc[m][n]);
#pragma unroll
    for (int u = 0; u < 9; u++) { __builtin_amdgcn_sched_group_barrier(0x8, 2, 0); __builtin_amdgcn_sched_group_barrier(0x100, 1, 0); }
    __builtin_amdgcn_sched_group_barrier(0x8, 2, 0);
  };
#pragma unroll 1
  for (int s = 0; s < 15; s++) {
    half1();
    slot = slot == (NS2 - 1) * STG2 ? 0 : slot + STG2;
    waitl0_bar();
    const unsigned an = a0 + slot, bn = b0 + slot;
#pragma unroll
    for (int m = 0; m < 4; m++) A0[m] = ldsr(an + m * 2048);
#pragma unroll
    for (int n = 0; n < 5; n++) B0[n] = ldsr(bn + n * 2048);
#pragma unroll
    for (int n = 0; n < 5; n++)
#pragma unroll
      for (int m = 0; m < 4; m++) acc[m][n] = MFMA(B1[n], A1[m], acc[m][n]);
#pragma unroll
    for (int u = 0; u < 9; u++) { __builtin_amdgcn_sched_group_barrier(0x8, 2, 1); __builtin_amdgcn_sched_group_barrier(0x100, 1, 1); }
    __builtin_amdgcn_sched_group_barrier(0x8, 2, 1);
  }
  half1();
#pragma unroll
  for (int n = 0; n < 5; n++)
#pragma unroll
    for (int m = 0; m < 4; m++) acc[m][n] = MFMA(B1[n], A1[m], acc[m][n]);
  ST(2);
  const __amdgpu_buffer_rsrc_t sX = mkrsrc(ws + XCH_OFF, XCH_BYTES + FLAG_BYTES);
  const int tile = tb * 4 + et;
#pragma unroll
  for (int j = 1; j < 4; j++) {
    const unsigned o = (((tile * 4 + ((j + q) & 3)) * 3 + (j - 1)) * 4 + w) * 2560;
    unsigned p[10];
#pragma unroll
    for (int n = 0; n < 5; n++) { p[2 * n] = pk(acc[j][n][0], acc[j][n][1]); p[2 * n + 1] = pk(acc[j][n][2], acc[j][n][3]); }
    __builtin_amdgcn_raw_buffer_store_b128((u32x4){p[0], p[1], p[2], p[3]}, sX, lane * 16, o, 16);
    __builtin_amdgcn_raw_buffer_store_b128((u32x4){p[4], p[5], p[6], p[7]}, sX, lane * 16, o + 1024, 16);
    __builtin_amdgcn_raw_buffer_store_b64((u32x2){p[8], p[9]}, sX, lane * 8, o + 2048, 16);
  }
  waitv<0>();
  ST(3);
  if (lane < 3) {
    const unsigned fo = XCH_BYTES + ((tile * 4 + ((lane + 1 + q) & 3)) * 4 + w) * 64 + lane * 4;
    __builtin_amdgcn_raw_buffer_store_b32(1u, sX, fo, 0, 16);
  }
  {
    const unsigned fo = XCH_BYTES + ((tile * 4 + q) * 4 + w) * 64;
#pragma unroll 1
    for (unsigned spin = 0; spin < (1u << 20); spin++) {
      u32x4 f = __builtin_amdgcn_raw_buffer_load_b128(sX, 0, fo, 16);
      if (rfl(f[0] & f[1] & f[2]) == 1u) break;
      __builtin_amdgcn_s_sleep(1);
    }
    asm volatile("" ::: "memory");
  }
  ST(4);
  u32x4 pa[3], pb[3]; u32x2 pc[3];
#pragma unroll
  for (int j = 0; j < 3; j++) {
    const unsigned o = (((tile * 4 + q) * 3 + j) * 4 + w) * 2560;
    pa[j] = __builtin_amdgcn_raw_buffer_load_b128(sX, lane * 16, o, 16);
    pb[j] = __builtin_amdgcn_raw_buffer_load_b128(sX, lane * 16, o + 1024, 16);
    pc[j] = __builtin_amdgcn_raw_buffer_load_b64(sX, lane * 8, o + 2048, 16);
  }
  unsigned r[10];
#pragma unroll
  for (int n = 0; n < 5; n++) {
    float v0 = acc[0][n][0], v1 = acc[0][n][1], v2 = acc[0][n][2], v3 = acc[0][n][3];
#pragma unroll
    for (int j = 0; j < 3; j++) {
      const unsigned u0 = n < 2 ? pa[j][2 * n] : (n < 4 ? pb[j][2 * n - 4] : pc[j][0]);
      const unsigned u1 = n < 2 ? pa[j][2 * n + 1] : (n < 4 ? pb[j][2 * n - 3] : pc[j][1]);
      v0 += lo16(u0); v1 += hi16(u0); v2 += lo16(u1); v3 += hi16(u1);
    }
    r[2 * n] = pk(v0, v1); r[2 * n + 1] = pk(v2, v3);
  }
  char* yrow = y + (tb * 128 + wm * 64 + q * 16 + fr) * 1280 + (et * 160 + wn * 80) * 2;
#pragma unroll
  for (int n = 0; n < 4; n += 2) {
    auto r0 = __builtin_amdgcn_permlane16_swap(r[2 * n], r[2 * n + 2], false, false);
    auto r1 = __builtin_amdgcn_permlane16_swap(r[2 * n + 1], r[2 * n + 3], false, false);
    *(u32x4*)(yrow + (n + (g & 1)) * 32 + (g >> 1) * 16) = (u32x4){r0[0], r1[0], r0[1], r1[1]};
  }
  *(u32x2*)(yrow + 128 + g * 8) = (u32x2){r[8], r[9]};
  ST(5);
}

extern "C" void kernel_launch(void* const* d_in, const int* in_sizes, int n_in, void* d_out, int out_size, void* d_ws, size_t ws_size, hipStream_t stream) {
  (void)hipFuncSetAttribute((const void*)k_stage1, hipFuncAttributeMaxDynamicSharedMemorySize, NS1 * STG1);
  (void)hipFuncSetAttribute((const void*)k_stage2, hipFuncAttributeMaxDynamicSharedMemorySize, NS2 * STG2);
  k_stage1<<<256, 512, NS1 * STG1, stream>>>((const char*)d_in[0], (const char*)d_in[3], (const char*)d_in[4], (char*)d_ws);
  k_stage2<<<256, 512, NS2 * STG2, stream>>>((const char*)d_in[1], (const char*)d_in[2], (char*)d_ws, (char*)d_out);
}
```

```cpp
#include <hip/hip_runtime.h>
#include <hip/hip_bf16.h>

typedef __attribute__((ext_vector_type(8))) __bf16 bf16x8;
typedef __attribute__((ext_vector_type(2))) __bf16 bf16x2;
typedef __attribute__((ext_vector_type(4))) float f32x4;
typedef __attribute__((ext_vector_type(2))) float f32x2;
typedef __attribute__((ext_vector_type(4))) unsigned u32x4;
typedef __attribute__((ext_vector_type(2))) unsigned u32x2;
typedef unsigned long long u64;
#define DI __device__ __forceinline__
#define LAS __attribute__((address_space(3)))

#define ROT1 1
#define ROT2 1

constexpr int S_ = 2048, D_ = 640;
constexpr unsigned AB_BYTES = 4u * 4 * 16 * 20480;
constexpr unsigned XCH_OFF = AB_BYTES;
constexpr unsigned XCH_BYTES = 256u * 3 * 4 * 2560;
constexpr unsigned FLAG_OFF = XCH_OFF + XCH_BYTES;
constexpr unsigned FLAG_BYTES = 256u * 256;
constexpr unsigned STAMP_OFF = FLAG_OFF + FLAG_BYTES;

DI unsigned rfl(unsigned x) { return __builtin_amdgcn_readfirstlane(x); }
DI __amdgpu_buffer_rsrc_t mkrsrc(const void* p, unsigned bytes) { return __builtin_amdgcn_make_buffer_rsrc((void*)p, 0, bytes, 0x00020000); }
template <int OFF> DI void dma(unsigned voff, __amdgpu_buffer_rsrc_t srd, unsigned soff) {
  asm volatile("buffer_load_dwordx4 %0, %1, %2 offen offset:%3 lds" ::"v"(voff), "s"(srd), "s"(soff), "n"(OFF) : "memory");
}
DI void setm0(unsigned m) { asm volatile("s_mov_b32 m0, %0\n\ts_nop 0" ::"s"(m) : "memory"); }
template <int N> DI void waitv() { asm volatile("s_waitcnt vmcnt(%0)" ::"n"(N) : "memory"); }
DI void bar() { asm volatile("s_barrier" ::: "memory"); }
DI void waitl0_bar() { asm volatile("s_waitcnt lgkmcnt(0)\n\ts_barrier" ::: "memory"); }
DI bf16x8 ldsr(unsigned addr) { return *(const LAS bf16x8*)(size_t)addr; }
DI unsigned pk(float a, float b) { f32x2 v = {a, b}; bf16x2 h = __builtin_convertvector(v, bf16x2); return __builtin_bit_cast(unsigned, h); }
DI float lo16(unsigned u) { return __uint_as_float(u << 16); }
DI float hi16(unsigned u) { return __uint_as_float(u & 0xffff0000u); }
#define MFMA(a, b, c) __builtin_amdgcn_mfma_f32_16x16x32_bf16(a, b, c, 0, 0, 0)

#define ST_DECL
#define ST(k)
#define STC(k)
#define ST_END

constexpr int NS1 = 5, STG1 = 26 * 1024;
__global__ __launch_bounds__(512, 2) void k_stage1(const char* __restrict__ x, const char* __restrict__ wre, const char* __restrict__ wim, char* __restrict__ ws) {
  constexpr int KID = 0; ST_DECL;
  ST(0);
  const int lane = threadIdx.x & 63, w = rfl(threadIdx.x >> 6);
  const int b = blockIdx.x, xcd = b & 7, i = b >> 3;
  const int q = xcd >> 1, part = q >> 1, sh = q & 1, th = xcd & 1, sb = i >> 3, es = i & 7;
  const int s0 = sh * 1024 + th * 512 + sb * 128;
  if (w >= 4) {
    const int lw = w - 4;
    const __amdgpu_buffer_rsrc_t sX = mkrsrc(x, S_ * D_ * 2), sW = mkrsrc(part ? wim : wre, D_ * D_ * 2);
    unsigned va[4], vb[3];
#pragma unroll
    for (int p = 0; p < 4; p++) { int r = (lw * 4 + p) * 8 + (lane >> 3); va[p] = (s0 + r) * 1280 + (((lane & 7) ^ ((r >> 1) & 7)) * 16) - p * 1024; }
    const int nb0 = lw < 2 ? lw * 3 : 6 + (lw - 2) * 2;
#pragma unroll
    for (int p = 0; p < 3; p++) { int r = (nb0 + p) * 8 + (lane >> 3); if (r > 79) r = 79; vb[p] = (es * 80 + r) * 1280 + (((lane & 7) ^ ((r >> 1) & 7)) * 16) - p * 1024; }
    int k = ROT1 ? (es + 2 * sb) % 10 : 0;
    unsigned slot = 0;
    auto issue = [&]() {
      const unsigned ko = k * 128;
      setm0(slot + lw * 4096);
      dma<0>(va[0], sX, ko); dma<1024>(va[1], sX, ko); dma<2048>(va[2], sX, ko); dma<3072>(va[3], sX, ko);
      setm0(slot + 16384 + nb0 * 1024);
      dma<0>(vb[0], sW, ko); dma<1024>(vb[1], sW, ko);
      if (lw < 2) dma<2048>(vb[2], sW, ko);
      k = k == 9 ? 0 : k + 1;
      slot = slot == (NS1 - 1) * STG1 ? 0 : slot + STG1;
    };
    if (lw == 0) ((unsigned*)(ws + FLAG_OFF))[b * 64 + lane] = 0;
    for (int s = 0; s < NS1; s++) issue();
    if (lw < 2) waitv<7 * (NS1 - 1)>(); else waitv<6 * (NS1 - 1)>();
    bar();
#pragma unroll 1
    for (int s = 0; s < 10 - NS1; s++) {
      if (lw < 2) waitv<7 * (NS1 - 2)>(); else waitv<6 * (NS1 - 2)>();
      bar();
      issue();
    }
    if (lw < 2) { waitv<7 * 3>(); bar(); waitv<7 * 2>(); bar(); waitv<7>(); bar(); waitv<0>(); bar(); }
    else        { waitv<6 * 3>(); bar(); waitv<6 * 2>(); bar(); waitv<6>(); bar(); waitv<0>(); bar(); }
    ST(1); ST_END;
    return;
  }
  const int fr = lane & 15, g = lane >> 4;
  const unsigned sw = (unsigned)((g ^ (fr >> 1)) * 16);
  const unsigned a0 = (w * 32 + fr) * 128 + sw, b0 = 16384 + fr * 128 + sw;
  f32x4 acc[2][5] = {};
  bf16x8 A0[2], B0[5], A1[2], B1[5];
  unsigned slot = 0;
  asm volatile("" ::"s"(ws));
  bar();
  ST(1); STC(6);
#pragma unroll
  for (int m = 0; m < 2; m++) A0[m] = ldsr(a0 + m * 2048);
#pragma unroll
  for (int n = 0; n < 5; n++) B0[n] = ldsr(b0 + n * 2048);
  auto half1 = [&]() {
    const unsigned a1 = (a0 ^ 64) + slot, b1 = (b0 ^ 64) + slot;
#pragma unroll
    for (int m = 0; m < 2; m++) A1[m] = ldsr(a1 + m * 2048);
#pragma unroll
    for (int n = 0; n < 5; n++) B1[n] = ldsr(b1 + n * 2048);
#pragma unroll
    for (int n = 0; n < 5; n++)
#pragma unroll
      for (int m = 0; m < 2; m++) acc[m][n] = MFMA(A0[m], B0[n], acc[m][n]);
#pragma unroll
    for (int u = 0; u < 7; u++) { __builtin_amdgcn_sched_group_barrier(0x8, 1, 0); __builtin_amdgcn_sched_group_barrier(0x100, 1, 0); }
    __builtin_amdgcn_sched_group_barrier(0x8, 3, 0);
  };
#pragma unroll 1
  for (int s = 0; s < 9; s++) {
    half1();
    slot = slot == (NS1 - 1) * STG1 ? 0 : slot + STG1;
    waitl0_bar();
    const unsigned an = a0 + slot, bn = b0 + slot;
#pragma unroll
    for (int m = 0; m < 2; m++) A0[m] = ldsr(an + m * 2048);
#pragma unroll
    for (int n = 0; n < 5; n++) B0[n] = ldsr(bn + n * 2048);
#pragma unroll
    for (int n = 0; n < 5; n++)
#pragma unroll
      for (int m = 0; m < 2; m++) acc[m][n] = MFMA(A1[m], B1[n], acc[m][n]);
#pragma unroll
    for (int u = 0; u < 7; u++) { __builtin_amdgcn_sched_group_barrier(0x8, 1, 1); __builtin_amdgcn_sched_group_barrier(0x100, 1, 1); }
    __builtin_amdgcn_sched_group_barrier(0x8, 3, 1);
  }
  half1();
#pragma unroll
  for (int n = 0; n < 5; n++)
#pragma unroll
    for (int m = 0; m < 2; m++) acc[m][n] = MFMA(A1[m], B1[n], acc[m][n]);
  ST(2); STC(7);
  const float sg = part ? -1.f : 1.f;
  const int sq = th * 512 + sb * 128 + w * 32;
  char* dst = ws + (((q * 4 + (es >> 1)) * 16 + (sq >> 6)) * 10 + (es & 1) * 5) * 2048 + ((sq >> 5) & 1) * 1024 + ((2 * (g & 1) + (g >> 1)) * 16 + fr) * 16;
#pragma unroll
  for (int n = 0; n < 5; n++) {
    unsigned x0 = pk(sg * acc[0][n][0], sg * acc[0][n][1]), x1 = pk(sg * acc[0][n][2], sg * acc[0][n][3]);
    unsigned y0 = pk(sg * acc[1][n][0], sg * acc[1][n][1]), y1 = pk(sg * acc[1][n][2], sg * acc[1][n][3]);
    auto r0 = __builtin_amdgcn_permlane16_swap(x0, y0, false, false);
    auto r1 = __builtin_amdgcn_permlane16_swap(x1, y1, false, false);
    *(u32x4*)(dst + n * 2048) = (u32x4){r0[0], r1[0], r0[1], r1[1]};
  }
  ST(3); ST_END;
}

constexpr int NS2 = 4, STG2 = 36 * 1024;
__global__ __launch_bounds__(512, 2) void k_stage2(const char* __restrict__ sre, const char* __restrict__ sim, char* __restrict__ ws, char* __restrict__ y) {
  constexpr int KID = 1; ST_DECL;
  ST(0);
  const int lane = threadIdx.x & 63, w = rfl(threadIdx.x >> 6);
  const int b = blockIdx.x, xcd = b & 7, i = b >> 3;
  const int q = xcd >> 1, part = q >> 1, sh = q & 1, th = xcd & 1, tb = th * 8 + (i >> 2), et = i & 3;
  if (w >= 4) {
    const int lw = w - 4;
    const __amdgpu_buffer_rsrc_t sA = mkrsrc(part ? sim : sre, S_ * S_ * 2), sB = mkrsrc(ws, AB_BYTES);
    unsigned va[4];
#pragma unroll
    for (int p = 0; p < 4; p++) {
      int r = (lw * 4 + p) * 8 + (lane >> 3);
      int t = tb * 128 + (r & 64) + ((((r >> 4) + q) & 3) << 4) + (r & 15);
      va[p] = t * 4096 + sh * 2048 + (((lane & 7) ^ ((r >> 1) & 7)) * 16) - p * 1024;
    }
    const unsigned vb = lane * 16 + lw * 5120;
    const unsigned bbase = (q * 4 + et) * 16 * 20480;
    int k = ROT2 ? ((et + (i >> 2)) & 3) : 0, kb = 0;
    unsigned slot = 0;
    auto issue = [&]() {
      const int ks = kb + k;
      const unsigned ao = ks * 128, bo = bbase + ks * 20480;
      setm0(slot + lw * 4096);
      dma<0>(va[0], sA, ao); dma<1024>(va[1], sA, ao); dma<2048>(va[2], sA, ao); dma<3072>(va[3], sA, ao);
      setm0(slot + 16384 + lw * 5120);
      dma<0>(vb, sB, bo); dma<1024>(vb, sB, bo); dma<2048>(vb, sB, bo); dma<3072>(vb, sB, bo);
      setm0(slot + 16384 + lw * 5120 + 4096);
      dma<0>(vb, sB, bo + 4096);
      k = (k + 1) & 3;
      slot = slot == (NS2 - 1) * STG2 ? 0 : slot + STG2;
      if (slot == 0) kb += 4;
    };
    for (int s = 0; s < NS2; s++) issue();
    waitv<9 * 3>(); bar();
#pragma unroll 1
    for (int s = 0; s < 16 - NS2; s++) { waitv<9 * 2>(); bar(); issue(); }
    waitv<9 * 2>(); bar(); waitv<9>(); bar(); waitv<0>(); bar();
    ST(1); ST_END;
    return;
  }
  const int fr = lane & 15, g = lane >> 4, wm = w >> 1, wn = w & 1;
  const unsigned a0 = (wm * 64 + fr) * 128 + (unsigned)((g ^ (fr >> 1)) * 16), b0 = 16384 + wn * 10240 + lane * 16;
  f32x4 acc[4][5] = {};
  bf16x8 A0[4], B0[5], A1[4], B1[5];
  unsigned slot = 0;
  asm volatile("" ::"s"(y), "s"(ws));
  bar();
  ST(1); STC(6);
#pragma unroll
  for (int m = 0; m < 4; m++) A0[m] = ldsr(a0 + m * 2048);
#pragma unroll
  for (int n = 0; n < 5; n++) B0[n] = ldsr(b0 + n * 2048);
  auto half1 = [&]() {
    const unsigned a1 = (a0 ^ 64) + slot, b1 = b0 + 1024 + slot;
#pragma unroll
    for (int m = 0; m < 4; m++) A1[m] = ldsr(a1 + m * 2048);
#pragma unroll
    for (int n = 0; n < 5; n++) B1[n] = ldsr(b1 + n * 2048);
#pragma unroll
    for (int n = 0; n < 5; n++)
#pragma unroll
      for (int m = 0; m < 4; m++) acc[m][n] = MFMA(B0[n], A0[m], acc[m][n]);
#pragma unroll
    for (int u = 0; u < 9; u++) { __builtin_amdgcn_sched_group_barrier(0x8, 2, 0); __builtin_amdgcn_sched_group_barrier(0x100, 1, 0); }
    __builtin_amdgcn_sched_group_barrier(0x8, 2, 0);
  };
#pragma unroll 1
  for (int s = 0; s < 15; s++) {
    half1();
    slot = slot == (NS2 - 1) * STG2 ? 0 : slot + STG2;
    waitl0_bar();
    const unsigned an = a0 + slot, bn = b0 + slot;
#pragma unroll
    for (int m = 0; m < 4; m++) A0[m] = ldsr(an + m * 2048);
#pragma unroll
    for (int n = 0; n < 5; n++) B0[n] = ldsr(bn + n * 2048);
#pragma unroll
    for (int n = 0; n < 5; n++)
#pragma unroll
      for (int m = 0; m < 4; m++) acc[m][n] = MFMA(B1[n], A1[m], acc[m][n]);
#pragma unroll
    for (int u = 0; u < 9; u++) { __builtin_amdgcn_sched_group_barrier(0x8, 2, 1); __builtin_amdgcn_sched_group_barrier(0x100, 1, 1); }
    __builtin_amdgcn_sched_group_barrier(0x8, 2, 1);
  }
  half1();
#pragma unroll
  for (int n = 0; n < 5; n++)
#pragma unroll
    for (int m = 0; m < 4; m++) acc[m][n] = MFMA(B1[n], A1[m], acc[m][n]);
  ST(2); STC(7);
  const __amdgpu_buffer_rsrc_t sX = mkrsrc(ws + XCH_OFF, XCH_BYTES + FLAG_BYTES);
  const int tile = tb * 4 + et;
#pragma unroll
  for (int j = 1; j < 4; j++) {
    const unsigned o = (((tile * 4 + ((j + q) & 3)) * 3 + (j - 1)) * 4 + w) * 2560;
    unsigned p[10];
#pragma unroll
    for (int n = 0; n < 5; n++) { p[2 * n] = pk(acc[j][n][0], acc[j][n][1]); p[2 * n + 1] = pk(acc[j][n][2], acc[j][n][3]); }
    __builtin_amdgcn_raw_buffer_store_b128((u32x4){p[0], p[1], p[2], p[3]}, sX, lane * 16, o, 16);
    __builtin_amdgcn_raw_buffer_store_b128((u32x4){p[4], p[5], p[6], p[7]}, sX, lane * 16, o + 1024, 16);
    __builtin_amdgcn_raw_buffer_store_b64((u32x2){p[8], p[9]}, sX, lane * 8, o + 2048, 16);
  }
  waitv<0>();
  ST(3);
  if (lane < 3) {
    const unsigned fo = XCH_BYTES + ((tile * 4 + ((lane + 1 + q) & 3)) * 4 + w) * 64 + lane * 4;
    __builtin_amdgcn_raw_buffer_store_b32(1u, sX, fo, 0, 16);
  }
  {
    const unsigned fo = XCH_BYTES + ((tile * 4 + q) * 4 + w) * 64;
#pragma unroll 1
    for (unsigned spin = 0; spin < (1u << 20); spin++) {
      u32x4 f = __builtin_amdgcn_raw_buffer_load_b128(sX, 0, fo, 16);
      if (rfl(f[0] & f[1] & f[2]) == 1u) break;
      __builtin_amdgcn_s_sleep(1);
    }
    asm volatile("" ::: "memory");
  }
  ST(4);
  u32x4 pa[3], pb[3]; u32x2 pc[3];
#pragma unroll
  for (int j = 0; j < 3; j++) {
    const unsigned o = (((tile * 4 + q) * 3 + j) * 4 + w) * 2560;
    pa[j] = __builtin_amdgcn_raw_buffer_load_b128(sX, lane * 16, o, 16);
    pb[j] = __builtin_amdgcn_raw_buffer_load_b128(sX, lane * 16, o + 1024, 16);
    pc[j] = __builtin_amdgcn_raw_buffer_load_b64(sX, lane * 8, o + 2048, 16);
  }
  unsigned r[10];
#pragma unroll
  for (int n = 0; n < 5; n++) {
    float v0 = acc[0][n][0], v1 = acc[0][n][1], v2 = acc[0][n][2], v3 = acc[0][n][3];
#pragma unroll
    for (int j = 0; j < 3; j++) {
      const unsigned u0 = n < 2 ? pa[j][2 * n] : (n < 4 ? pb[j][2 * n - 4] : pc[j][0]);
      const unsigned u1 = n < 2 ? pa[j][2 * n + 1] : (n < 4 ? pb[j][2 * n - 3] : pc[j][1]);
      v0 += lo16(u0); v1 += hi16(u0); v2 += lo16(u1); v3 += hi16(u1);
    }
    r[2 * n] = pk(v0, v1); r[2 * n + 1] = pk(v2, v3);
  }
  char* yrow = y + (tb * 128 + wm * 64 + q * 16 + fr) * 1280 + (et * 160 + wn * 80) * 2;
#pragma unroll
  for (int n = 0; n < 4; n += 2) {
    auto r0 = __builtin_amdgcn_permlane16_swap(r[2 * n], r[2 * n + 2], false, false);
    auto r1 = __builtin_amdgcn_permlane16_swap(r[2 * n + 1], r[2 * n + 3], false, false);
    *(u32x4*)(yrow + (n + (g & 1)) * 32 + (g >> 1) * 16) = (u32x4){r0[0], r1[0], r0[1], r1[1]};
  }
  *(u32x2*)(yrow + 128 + g * 8) = (u32x2){r[8], r[9]};
  ST(5); ST_END;
}

extern "C" void kernel_launch(void* const* d_in, const int* in_sizes, int n_in, void* d_out, int out_size, void* d_ws, size_t ws_size, hipStream_t stream) {
  (void)hipFuncSetAttribute((const void*)k_stage1, hipFuncAttributeMaxDynamicSharedMemorySize, NS1 * STG1);
  (void)hipFuncSetAttribute((const void*)k_stage2, hipFuncAttributeMaxDynamicSharedMemorySize, NS2 * STG2);
  k_stage1<<<256, 512, NS1 * STG1, stream>>>((const char*)d_in[0], (const char*)d_in[3], (const char*)d_in[4], (char*)d_ws);
  k_stage2<<<256, 512, NS2 * STG2, stream>>>((const char*)d_in[1], (const char*)d_in[2], (char*)d_ws, (char*)d_out);
}
```

```cpp
#include <hip/hip_runtime.h>
#include <hip/hip_bf16.h>

typedef __attribute__((ext_vector_type(2))) __bf16 bf16x2;
typedef __attribute__((ext_vector_type(2))) float f32x2;
typedef __attribute__((ext_vector_type(4))) float f32x4;
typedef __attribute__((ext_vector_type(8))) float f32x8;
typedef __attribute__((ext_vector_type(16))) float f32x16;
typedef __attribute__((ext_vector_type(32))) float f32x32;
typedef __attribute__((ext_vector_type(4))) unsigned u32x4;
typedef __attribute__((ext_vector_type(2))) unsigned u32x2;
typedef unsigned long long u64;
typedef __amdgpu_buffer_rsrc_t rsrc_t;
#define DI __device__ __forceinline__

#define ROT1 5
#define NOCLR 0
#define NOLOAD 0
#define APOS 1
#define PSLEEP 2
#define FSLEEP 4
#define CLS 2
#define NPRE 2
#define CLN 15
#define CLD 3
#define CLZ 4
#define SL1 4
#define SL2 8
#define SL3 6
#define DIST 4
#define DRAMP 0
#define BFIRST 1
#define LSTEP 0
#define MSTEP 2
#define TRS 0
#define NOCOMP1 0
#define CLRPOS 2
#define YAUX 16
#define ALLBY 12
#define CYC1 0
#define NOCOMP2 0
#define PLAINB 0
#define PRE1 0
#define LPRIO 3
#define XSLEEP 1
#define CODEPF 1

constexpr int S_ = 2048, D_ = 640;
constexpr unsigned AB_BYTES = 4u * 4 * 16 * 20480;
constexpr unsigned XCH_OFF = AB_BYTES;
constexpr unsigned XCH_WG = 3 * 4 * 2560;
constexpr unsigned FL_OFF = XCH_OFF + 256 * XCH_WG;
constexpr unsigned FL_STRIDE = 64;
constexpr unsigned REQ_OFF = FL_OFF + 32768, ECHO_OFF = REQ_OFF + 4096;
constexpr unsigned WS_BYTES = ECHO_OFF + 4096;
constexpr unsigned STAMP_OFF = WS_BYTES;
constexpr unsigned CODE_BYTES = 18048;
constexpr unsigned SPIN_MAX = 1u << 16;
constexpr unsigned LDS_BYTES = 163840;
constexpr unsigned WB = 49152;
constexpr unsigned LB_BASE = 65536;

DI unsigned rfl(unsigned x) { return __builtin_amdgcn_readfirstlane(x); }
DI rsrc_t mkrsrc(const void* p, unsigned bytes) { return __builtin_amdgcn_make_buffer_rsrc((void*)p, 0, bytes, 0x00020000); }
DI void pair(const char* base, unsigned lds, unsigned va, unsigned vb) {
  if (NOLOAD) return;
  asm volatile("s_mov_b32 m0, %1\n\ts_nop 0\n\tglobal_load_lds_dwordx4 %2, %0\n\tglobal_load_lds_dwordx4 %3, %0 offset:1024" ::"s"(base), "s"(lds), "v"(va), "v"(vb) : "memory");
}
DI void pair_c(const char* base, unsigned lds, unsigned v) {
  if (NOLOAD) return;
  asm volatile("s_mov_b32 m0, %1\n\ts_nop 0\n\tglobal_load_lds_dwordx4 %2, %0 sc1\n\tglobal_load_lds_dwordx4 %2, %0 offset:1024 sc1" ::"s"(base), "s"(lds), "v"(v) : "memory");
}
DI unsigned vm_count() { return __builtin_amdgcn_s_getreg(7 | (0 << 6) | (3 << 11)) | (__builtin_amdgcn_s_getreg(7 | (22 << 6) | (1 << 11)) << 4); }
template <int V> struct IC { static constexpr int value = V; };
DI void setm0(unsigned m) { asm volatile("s_mov_b32 m0, %0\n\ts_nop 0" ::"s"(m) : "memory"); }
template <int N> DI void waitv() { asm volatile("s_waitcnt vmcnt(%0)" ::"n"(N) : "memory"); }
DI void bar() { asm volatile("s_barrier" ::: "memory"); }
DI bool all_lanes(bool c) { return __builtin_amdgcn_ballot_w64(!c) == 0; }
DI unsigned pk(float a, float b) { f32x2 v = {a, b}; bf16x2 h = __builtin_convertvector(v, bf16x2); return __builtin_bit_cast(unsigned, h); }
DI float lo16(unsigned u) { return __uint_as_float(u << 16); }
DI float hi16(unsigned u) { return __uint_as_float(u & 0xffff0000u); }
DI void st128(u32x4 v, rsrc_t r, unsigned o) { __builtin_amdgcn_raw_buffer_store_b128(v, r, o, 0, 16); }
DI void st64(u32x2 v, rsrc_t r, unsigned o) { __builtin_amdgcn_raw_buffer_store_b64(v, r, o, 0, 16); }
DI void st32(unsigned v, rsrc_t r, unsigned o) { __builtin_amdgcn_raw_buffer_store_b32(v, r, o, 0, 16); }
DI unsigned ld32(rsrc_t r, unsigned o) { return __builtin_amdgcn_raw_buffer_load_b32(r, o, 0, 16); }
DI u32x2 ld64(rsrc_t r, unsigned o) { return __builtin_amdgcn_raw_buffer_load_b64(r, o, 0, 16); }
DI u32x4 ld128(rsrc_t r, unsigned o) { return __builtin_amdgcn_raw_buffer_load_b128(r, o, 0, 16); }

#define ST_DECL
#define ST(k)
#define STC(k)
#define ST_END
#define TR(i)
#define TR_END

typedef __attribute__((ext_vector_type(4))) u64 u64x4;
typedef __attribute__((ext_vector_type(2))) u64 u64x2;
#define ARGS_GO \
  u64x4 ka; u64x2 kb; u64 kc; \
  asm volatile("s_load_dwordx8 %0, %3, 0x0\n\ts_load_dwordx4 %1, %3, 0x20\n\ts_load_dwordx2 %2, %3, 0x30" \
               : "=&{s[76:83]}"(ka), "=&{s[84:87]}"(kb), "=&{s[88:89]}"(kc) : "s"(__builtin_amdgcn_kernarg_segment_ptr())); \
  __builtin_amdgcn_sched_barrier(0)
#define IDS \
  int b = blockIdx.x; asm volatile("" : "+s"(b)); \
  const int xcd = b & 7, i = b >> 3; \
  const int q = xcd >> 1, part = q >> 1, sh = q & 1, th = xcd & 1;         \
  const int sb = i >> 3, es = i & 7;                                        \
  const int tb = th * 8 + (i >> 2), et = i & 3;                             \
    \
  const u64 seq1 = (u64)(((0x43214321u >> (4 * ((es + sb) & 3))) & 0xffff) << 4) | (u64)((unsigned)(0x321043210ull >> (4 * (es >= 5 ? es - 5 : es))) & 0xfffff) << 20; \
  const unsigned myline = FL_OFF + (q * 64 + (es >> 1) * 16 + (th * 4 + sb) * 2 + (es & 1)) * FL_STRIDE; \
  (void)sh
#define ARGS \
  asm volatile("s_waitcnt lgkmcnt(0)" : "+{s[76:83]}"(ka), "+{s[84:87]}"(kb), "+{s[88:89]}"(kc)); \
  const char *const x = (const char*)ka[0], *const sre = (const char*)ka[1], *const sim = (const char*)ka[2], *const wre = (const char*)ka[3], *const wim = (const char*)kb[0]; \
  char *const ws = (char*)kb[1], *const y = (char*)kc; \
  (void)x; (void)sre; (void)sim; (void)wre; (void)wim; (void)y

__global__ __launch_bounds__(512, 2) void fnet_fused(const char* x_, const char* sre_, const char* sim_, const char* wre_, const char* wim_, char* ws_, char* y_) {
  u64 pc0; asm volatile("s_getpc_b64 %0" : "=s"(pc0));
  ST_DECL; ST(0);
  const int lane = threadIdx.x & 63, w = rfl(threadIdx.x >> 6);

  if (w < 4) {
  ARGS_GO;
  if (CODEPF) {
    const char* cp = (const char*)(pc0 & ~(u64)255) + min(((blockIdx.x >> 3) * 8 + w * 2 + (lane & 1)) * 128u, CODE_BYTES - 128u);
    setm0(152064);
    asm volatile("global_load_lds_dword %0, off" ::"v"(cp) : "memory");
    __builtin_amdgcn_sched_barrier(0);
  }
  ARGS;
  const rsrc_t sW = mkrsrc(ws, WS_BYTES);
  IDS;
  const int fr = lane & 15, g = lane >> 4, wm = w >> 1, wn = w & 1;
  const unsigned swz = (unsigned)((g ^ (fr >> 1)) * 16);
  const unsigned xa0 = (wm * 32 + fr) * 128 + swz, wb0 = WB + (wn * 48 + fr) * 128 + swz;
  const unsigned dvo = (((q * 4 + (es >> 1)) * 16 + th * 4 + sb) * 10 + (es & 1) * 5 + wn * 3) * 2048 + wm * 1024 + ((2 * (g & 1) + (g >> 1)) * 16 + fr) * 16;
  const unsigned a0 = (wm * 64 + fr) * 128 + swz, b0 = LB_BASE + wn * 10240 + lane * 16;
  const int tile = tb * 4 + et;
  unsigned xo[4];
#pragma unroll
  for (int j = 0; j < 4; j++) xo[j] = XCH_OFF + (tile * 4 + ((j + q) & 3)) * XCH_WG + ((j ? j - 1 : 0) * 4 + w) * 2560;
  const unsigned myw = myline + 16 + w * 4, yo = (tb * 128 + wm * 64 + q * 16 + fr) * 1280 + (et * 160 + wn * 80) * 2;
  const rsrc_t sY = mkrsrc(y, S_ * D_ * 2);
  asm volatile(
    "v_mov_b64 v[0:1], 0\n"
    "v_mov_b64 v[2:3], 0\n"
    "v_mov_b64 v[4:5], 0\n"
    "v_mov_b64 v[6:7], 0\n"
    "v_mov_b64 v[8:9], 0\n"
    "v_mov_b64 v[10:11], 0\n"
    "v_mov_b64 v[12:13], 0\n"
    "v_mov_b64 v[14:15], 0\n"
    "v_mov_b64 v[16:17], 0\n"
    "v_mov_b64 v[18:19], 0\n"
    "v_mov_b64 v[20:21], 0\n"
    "v_mov_b64 v[22:23], 0\n"
    "v_mov_b64 v[24:25], 0\n"
    "v_mov_b64 v[26:27], 0\n"
    "v_mov_b64 v[28:29], 0\n"
    "v_mov_b64 v[30:31], 0\n"
    "v_mov_b64 v[32:33], 0\n"
    "v_mov_b64 v[34:35], 0\n"
    "v_mov_b64 v[36:37], 0\n"
    "v_mov_b64 v[38:39], 0\n"
    "v_mov_b64 v[40:41], 0\n"
    "v_mov_b64 v[42:43], 0\n"
    "v_mov_b64 v[44:45], 0\n"
    "v_mov_b64 v[46:47], 0\n"
    "v_mov_b64 v[48:49], 0\n"
    "v_mov_b64 v[50:51], 0\n"
    "v_mov_b64 v[52:53], 0\n"
    "v_mov_b64 v[54:55], 0\n"
    "v_mov_b64 v[56:57], 0\n"
    "v_mov_b64 v[58:59], 0\n"
    "v_mov_b64 v[60:61], 0\n"
    "v_mov_b64 v[62:63], 0\n"
    "v_mov_b64 v[64:65], 0\n"
    "v_mov_b64 v[66:67], 0\n"
    "v_mov_b64 v[68:69], 0\n"
    "v_mov_b64 v[70:71], 0\n"
    "v_mov_b64 v[72:73], 0\n"
    "v_mov_b64 v[74:75], 0\n"
    "v_mov_b64 v[76:77], 0\n"
    "v_mov_b64 v[78:79], 0\n"
    "v_mov_b64 v[164:165], 0\n"
    "v_mov_b64 v[166:167], 0\n"
    "v_mov_b64 v[168:169], 0\n"
    "v_mov_b64 v[170:171], 0\n"
    "v_mov_b64 v[172:173], 0\n"
    "v_mov_b64 v[174:175], 0\n"
    "v_mov_b64 v[176:177], 0\n"
    "v_mov_b64 v[178:179], 0\n"
    "v_mov_b64 v[180:181], 0\n"
    "v_mov_b64 v[182:183], 0\n"
    "v_mov_b64 v[184:185], 0\n"
    "v_mov_b64 v[186:187], 0\n"
    "v_mov_b64 v[188:189], 0\n"
    "v_mov_b64 v[190:191], 0\n"
    "v_mov_b64 v[192:193], 0\n"
    "v_mov_b64 v[194:195], 0\n"
    "v_mov_b64 v[196:197], 0\n"
    "v_mov_b64 v[198:199], 0\n"
    "v_mov_b64 v[200:201], 0\n"
    "v_mov_b64 v[202:203], 0\n"
    "v_mov_b64 v[204:205], 0\n"
    "v_mov_b64 v[206:207], 0\n"
    "v_mov_b64 v[208:209], 0\n"
    "v_mov_b64 v[210:211], 0\n"
    "s_movk_i32 s46, 4096\n"
    "s_mov_b32 s47, 163840\n"
    "s_mov_b32 s48, 167936\n"
    "s_cmp_lg_u32 %22, 0\n"
    "s_cbranch_scc1 .Ltwo%=\n"
    "s_mov_b64 s[44:45], %18\n"
    "s_mov_b32 s42, 0\n"
    "s_and_b32 s43, s44, 15\n"
    "s_mul_i32 s43, s43, 20480\n"
    "v_add_u32 v160, s42, %0\n"
    "v_add_u32 v161, s42, %1\n"
    "v_add_u32 v162, s43, %2\n"
    "v_add_u32 v163, s43, %3\n"
    "s_mov_b32 s40, 5\n"
    "s_barrier\n"
    "ds_read_b128 v[80:83], v160 offset:0\n"
    "ds_read_b128 v[84:87], v160 offset:2048\n"
    "ds_read_b128 v[112:115], v162 offset:0\n"
    "ds_read_b128 v[116:119], v162 offset:2048\n"
    "ds_read_b128 v[120:123], v162 offset:4096\n"
    "ds_read_b128 v[88:91], v161 offset:0\n"
    "ds_read_b128 v[92:95], v161 offset:2048\n"
    "ds_read_b128 v[124:127], v163 offset:0\n"
    "ds_read_b128 v[128:131], v163 offset:2048\n"
    "ds_read_b128 v[132:135], v163 offset:4096\n"
    ".Lloopa%=:\n"
    "s_waitcnt lgkmcnt(7)\n"
    "v_mfma_f32_16x16x32_bf16 v[164:167], v[80:83], v[112:115], v[164:167]\n"
    "ds_read_b128 v[96:99], v160 offset:8192\n"
    "v_mfma_f32_16x16x32_bf16 v[168:171], v[84:87], v[112:115], v[168:171]\n"
    "ds_read_b128 v[100:103], v160 offset:10240\n"
    "s_waitcnt lgkmcnt(8)\n"
    "v_mfma_f32_16x16x32_bf16 v[172:175], v[80:83], v[116:119], v[172:175]\n"
    "ds_read_b128 v[136:139], v162 offset:10240\n"
    "v_mfma_f32_16x16x32_bf16 v[176:179], v[84:87], v[116:119], v[176:179]\n"
    "ds_read_b128 v[140:143], v162 offset:12288\n"
    "s_waitcnt lgkmcnt(9)\n"
    "v_mfma_f32_16x16x32_bf16 v[180:183], v[80:83], v[120:123], v[180:183]\n"
    "ds_read_b128 v[144:147], v162 offset:14336\n"
    "v_mfma_f32_16x16x32_bf16 v[184:187], v[84:87], v[120:123], v[184:187]\n"
    "s_waitcnt lgkmcnt(5)\n"
    "v_mfma_f32_16x16x32_bf16 v[164:167], v[88:91], v[124:127], v[164:167]\n"
    "ds_read_b128 v[104:107], v161 offset:8192\n"
    "s_add_u32 s42, s42, 16384\n"
    "v_mfma_f32_16x16x32_bf16 v[168:171], v[92:95], v[124:127], v[168:171]\n"
    "ds_read_b128 v[108:111], v161 offset:10240\n"
    "s_lshr_b64 s[44:45], s[44:45], 4\n"
    "v_mfma_f32_16x16x32_bf16 v[172:175], v[88:91], v[128:131], v[172:175]\n"
    "ds_read_b128 v[148:151], v163 offset:10240\n"
    "s_cmp_eq_u32 s42, 49152\n"
    "s_cselect_b32 s42, 0, s42\n"
    "v_mfma_f32_16x16x32_bf16 v[176:179], v[92:95], v[128:131], v[176:179]\n"
    "ds_read_b128 v[152:155], v163 offset:12288\n"
    "s_and_b32 s43, s44, 15\n"
    "v_mfma_f32_16x16x32_bf16 v[180:183], v[88:91], v[132:135], v[180:183]\n"
    "ds_read_b128 v[156:159], v163 offset:14336\n"
    "s_mul_i32 s43, s43, 20480\n"
    "v_mfma_f32_16x16x32_bf16 v[184:187], v[92:95], v[132:135], v[184:187]\n"
    "s_waitcnt lgkmcnt(5)\n"
    "v_mfma_f32_16x16x32_bf16 v[164:167], v[96:99], v[136:139], v[164:167]\n"
    "v_add_u32 v160, s42, %0\n"
    "v_add_u32 v161, s42, %1\n"
    "v_mfma_f32_16x16x32_bf16 v[168:171], v[100:103], v[136:139], v[168:171]\n"
    "v_add_u32 v162, s43, %2\n"
    "v_add_u32 v163, s43, %3\n"
    "s_waitcnt lgkmcnt(0)\n"
    "s_barrier\n"
    "v_mfma_f32_16x16x32_bf16 v[172:175], v[96:99], v[140:143], v[172:175]\n"
    "ds_read_b128 v[80:83], v160 offset:0\n"
    "v_mfma_f32_16x16x32_bf16 v[176:179], v[100:103], v[140:143], v[176:179]\n"
    "ds_read_b128 v[84:87], v160 offset:2048\n"
    "v_mfma_f32_16x16x32_bf16 v[180:183], v[96:99], v[144:147], v[180:183]\n"
    "ds_read_b128 v[112:115], v162 offset:0\n"
    "v_mfma_f32_16x16x32_bf16 v[184:187], v[100:103], v[144:147], v[184:187]\n"
    "ds_read_b128 v[116:119], v162 offset:2048\n"
    "v_mfma_f32_16x16x32_bf16 v[164:167], v[104:107], v[148:151], v[164:167]\n"
    "ds_read_b128 v[120:123], v162 offset:4096\n"
    "v_mfma_f32_16x16x32_bf16 v[168:171], v[108:111], v[148:151], v[168:171]\n"
    "ds_read_b128 v[88:91], v161 offset:0\n"
    "v_mfma_f32_16x16x32_bf16 v[172:175], v[104:107], v[152:155], v[172:175]\n"
    "ds_read_b128 v[92:95], v161 offset:2048\n"
    "v_mfma_f32_16x16x32_bf16 v[176:179], v[108:111], v[152:155], v[176:179]\n"
    "ds_read_b128 v[124:127], v163 offset:0\n"
    "v_mfma_f32_16x16x32_bf16 v[180:183], v[104:107], v[156:159], v[180:183]\n"
    "ds_read_b128 v[128:131], v163 offset:2048\n"
    "v_mfma_f32_16x16x32_bf16 v[184:187], v[108:111], v[156:159], v[184:187]\n"
    "ds_read_b128 v[132:135], v163 offset:4096\n"
    "s_sub_u32 s40, s40, 1\n"
    "s_cmp_lg_u32 s40, 0\n"
    "s_cbranch_scc1 .Lloopa%=\n"
    "s_waitcnt lgkmcnt(7)\n"
    "v_mfma_f32_16x16x32_bf16 v[188:191], v[80:83], v[112:115], v[188:191]\n"
    "ds_read_b128 v[96:99], v160 offset:8192\n"
    "v_cvt_pk_bf16_f32 v164, v164, v165\n"
    "v_cvt_pk_bf16_f32 v165, v166, v167\n"
    "v_cvt_pk_bf16_f32 v166, v168, v169\n"
    "v_mfma_f32_16x16x32_bf16 v[192:195], v[84:87], v[112:115], v[192:195]\n"
    "ds_read_b128 v[100:103], v160 offset:10240\n"
    "v_cvt_pk_bf16_f32 v167, v170, v171\n"
    "v_permlane16_swap_b32 v164, v166\n"
    "s_nop 0\n"
    "v_permlane16_swap_b32 v165, v167\n"
    "s_waitcnt lgkmcnt(8)\n"
    "v_mfma_f32_16x16x32_bf16 v[196:199], v[80:83], v[116:119], v[196:199]\n"
    "ds_read_b128 v[136:139], v162 offset:10240\n"
    "v_xor_b32 v164, %17, v164\n"
    "v_xor_b32 v165, %17, v165\n"
    "v_xor_b32 v166, %17, v166\n"
    "v_mfma_f32_16x16x32_bf16 v[200:203], v[84:87], v[116:119], v[200:203]\n"
    "ds_read_b128 v[140:143], v162 offset:12288\n"
    "v_xor_b32 v167, %17, v167\n"
    "buffer_store_dwordx4 v[164:167], %4, %15, 0 offen offset:0 sc1\n"
    "v_cvt_pk_bf16_f32 v172, v172, v173\n"
    "s_waitcnt lgkmcnt(9)\n"
    "v_mfma_f32_16x16x32_bf16 v[204:207], v[80:83], v[120:123], v[204:207]\n"
    "ds_read_b128 v[144:147], v162 offset:14336\n"
    "v_cvt_pk_bf16_f32 v173, v174, v175\n"
    "v_cvt_pk_bf16_f32 v174, v176, v177\n"
    "v_cvt_pk_bf16_f32 v175, v178, v179\n"
    "v_mfma_f32_16x16x32_bf16 v[208:211], v[84:87], v[120:123], v[208:211]\n"
    "v_permlane16_swap_b32 v172, v174\n"
    "v_permlane16_swap_b32 v173, v175\n"
    "v_xor_b32 v172, %17, v172\n"
    "s_waitcnt lgkmcnt(5)\n"
    "v_mfma_f32_16x16x32_bf16 v[188:191], v[88:91], v[124:127], v[188:191]\n"
    "ds_read_b128 v[104:107], v161 offset:8192\n"
    "s_add_u32 s42, s42, 16384\n"
    "v_xor_b32 v173, %17, v173\n"
    "v_xor_b32 v174, %17, v174\n"
    "v_xor_b32 v175, %17, v175\n"
    "v_mfma_f32_16x16x32_bf16 v[192:195], v[92:95], v[124:127], v[192:195]\n"
    "ds_read_b128 v[108:111], v161 offset:10240\n"
    "s_lshr_b64 s[44:45], s[44:45], 4\n"
    "buffer_store_dwordx4 v[172:175], %4, %15, 0 offen offset:2048 sc1\n"
    "v_cvt_pk_bf16_f32 v180, v180, v181\n"
    "v_cvt_pk_bf16_f32 v181, v182, v183\n"
    "v_mfma_f32_16x16x32_bf16 v[196:199], v[88:91], v[128:131], v[196:199]\n"
    "ds_read_b128 v[148:151], v163 offset:10240\n"
    "s_cmp_eq_u32 s42, 49152\n"
    "s_cselect_b32 s42, 0, s42\n"
    "v_cvt_pk_bf16_f32 v182, v184, v185\n"
    "v_cvt_pk_bf16_f32 v183, v186, v187\n"
    "s_nop 0\n"
    "v_permlane16_swap_b32 v180, v182\n"
    "v_mfma_f32_16x16x32_bf16 v[200:203], v[92:95], v[128:131], v[200:203]\n"
    "ds_read_b128 v[152:155], v163 offset:12288\n"
    "s_and_b32 s43, s44, 15\n"
    "v_permlane16_swap_b32 v181, v183\n"
    "v_xor_b32 v180, %17, v180\n"
    "v_xor_b32 v181, %17, v181\n"
    "v_mfma_f32_16x16x32_bf16 v[204:207], v[88:91], v[132:135], v[204:207]\n"
    "ds_read_b128 v[156:159], v163 offset:14336\n"
    "s_mul_i32 s43, s43, 20480\n"
    "v_xor_b32 v182, %17, v182\n"
    "v_xor_b32 v183, %17, v183\n"
    "buffer_store_dwordx4 v[180:183], %4, %15, s46 offen offset:0 sc1\n"
    "v_mfma_f32_16x16x32_bf16 v[208:211], v[92:95], v[132:135], v[208:211]\n"
    "s_waitcnt lgkmcnt(5)\n"
    "v_mfma_f32_16x16x32_bf16 v[188:191], v[96:99], v[136:139], v[188:191]\n"
    "v_add_u32 v160, s42, %0\n"
    "v_add_u32 v161, s42, %1\n"
    "v_mfma_f32_16x16x32_bf16 v[192:195], v[100:103], v[136:139], v[192:195]\n"
    "v_add_u32 v162, s43, %2\n"
    "v_add_u32 v163, s43, %3\n"
    "s_waitcnt lgkmcnt(0)\n"
    "s_barrier\n"
    "v_mfma_f32_16x16x32_bf16 v[196:199], v[96:99], v[140:143], v[196:199]\n"
    "ds_read_b128 v[80:83], v160 offset:0\n"
    "v_mfma_f32_16x16x32_bf16 v[200:203], v[100:103], v[140:143], v[200:203]\n"
    "ds_read_b128 v[84:87], v160 offset:2048\n"
    "v_mfma_f32_16x16x32_bf16 v[204:207], v[96:99], v[144:147], v[204:207]\n"
    "ds_read_b128 v[112:115], v162 offset:0\n"
    "v_mfma_f32_16x16x32_bf16 v[208:211], v[100:103], v[144:147], v[208:211]\n"
    "ds_read_b128 v[116:119], v162 offset:2048\n"
    "v_mfma_f32_16x16x32_bf16 v[188:191], v[104:107], v[148:151], v[188:191]\n"
    "ds_read_b128 v[120:123], v162 offset:4096\n"
    "v_mfma_f32_16x16x32_bf16 v[192:195], v[108:111], v[148:151], v[192:195]\n"
    "ds_read_b128 v[88:91], v161 offset:0\n"
    "v_mfma_f32_16x16x32_bf16 v[196:199], v[104:107], v[152:155], v[196:199]\n"
    "ds_read_b128 v[92:95], v161 offset:2048\n"
    "v_mfma_f32_16x16x32_bf16 v[200:203], v[108:111], v[152:155], v[200:203]\n"
    "ds_read_b128 v[124:127], v163 offset:0\n"
    "v_mfma_f32_16x16x32_bf16 v[204:207], v[104:107], v[156:159], v[204:207]\n"
    "ds_read_b128 v[128:131], v163 offset:2048\n"
    "v_mfma_f32_16x16x32_bf16 v[208:211], v[108:111], v[156:159], v[208:211]\n"
    "ds_read_b128 v[132:135], v163 offset:4096\n"
    "s_waitcnt lgkmcnt(7)\n"
    "v_mfma_f32_16x16x32_bf16 v[188:191], v[80:83], v[112:115], v[188:191]\n"
    "ds_read_b128 v[96:99], v160 offset:8192\n"
    "v_mfma_f32_16x16x32_bf16 v[192:195], v[84:87], v[112:115], v[192:195]\n"
    "ds_read_b128 v[100:103], v160 offset:10240\n"
    "s_waitcnt lgkmcnt(8)\n"
    "v_mfma_f32_16x16x32_bf16 v[196:199], v[80:83], v[116:119], v[196:199]\n"
    "ds_read_b128 v[136:139], v162 offset:10240\n"
    "v_mfma_f32_16x16x32_bf16 v[200:203], v[84:87], v[116:119], v[200:203]\n"
    "ds_read_b128 v[140:143], v162 offset:12288\n"
    "s_waitcnt lgkmcnt(9)\n"
    "v_mfma_f32_16x16x32_bf16 v[204:207], v[80:83], v[120:123], v[204:207]\n"
    "ds_read_b128 v[144:147], v162 offset:14336\n"
    "v_mfma_f32_16x16x32_bf16 v[208:211], v[84:87], v[120:123], v[208:211]\n"
    "s_waitcnt lgkmcnt(5)\n"
    "v_mfma_f32_16x16x32_bf16 v[188:191], v[88:91], v[124:127], v[188:191]\n"
    "ds_read_b128 v[104:107], v161 offset:8192\n"
    "s_add_u32 s42, s42, 16384\n"
    "v_mfma_f32_16x16x32_bf16 v[192:195], v[92:95], v[124:127], v[192:195]\n"
    "ds_read_b128 v[108:111], v161 offset:10240\n"
    "s_lshr_b64 s[44:45], s[44:45], 4\n"
    "v_mfma_f32_16x16x32_bf16 v[196:199], v[88:91], v[128:131], v[196:199]\n"
    "ds_read_b128 v[148:151], v163 offset:10240\n"
    "s_cmp_eq_u32 s42, 49152\n"
    "s_cselect_b32 s42, 0, s42\n"
    "v_mfma_f32_16x16x32_bf16 v[200:203], v[92:95], v[128:131], v[200:203]\n"
    "ds_read_b128 v[152:155], v163 offset:12288\n"
    "s_and_b32 s43, s44, 15\n"
    "v_mfma_f32_16x16x32_bf16 v[204:207], v[88:91], v[132:135], v[204:207]\n"
    "ds_read_b128 v[156:159], v163 offset:14336\n"
    "s_mul_i32 s43, s43, 20480\n"
    "v_mfma_f32_16x16x32_bf16 v[208:211], v[92:95], v[132:135], v[208:211]\n"
    "s_waitcnt lgkmcnt(5)\n"
    "v_mfma_f32_16x16x32_bf16 v[188:191], v[96:99], v[136:139], v[188:191]\n"
    "v_add_u32 v160, s42, %0\n"
    "v_add_u32 v161, s42, %1\n"
    "v_mfma_f32_16x16x32_bf16 v[192:195], v[100:103], v[136:139], v[192:195]\n"
    "v_add_u32 v162, s43, %2\n"
    "v_add_u32 v163, s43, %3\n"
    "s_waitcnt lgkmcnt(0)\n"
    "s_waitcnt vmcnt(0)\n"
    "s_barrier\n"
    "v_mfma_f32_16x16x32_bf16 v[196:199], v[96:99], v[140:143], v[196:199]\n"
    "ds_read_b128 v[80:83], v160 offset:0\n"
    "v_mfma_f32_16x16x32_bf16 v[200:203], v[100:103], v[140:143], v[200:203]\n"
    "ds_read_b128 v[84:87], v160 offset:2048\n"
    "v_mfma_f32_16x16x32_bf16 v[204:207], v[96:99], v[144:147], v[204:207]\n"
    "ds_read_b128 v[112:115], v162 offset:0\n"
    "v_mfma_f32_16x16x32_bf16 v[208:211], v[100:103], v[144:147], v[208:211]\n"
    "ds_read_b128 v[116:119], v162 offset:2048\n"
    "v_mfma_f32_16x16x32_bf16 v[188:191], v[104:107], v[148:151], v[188:191]\n"
    "ds_read_b128 v[120:123], v162 offset:4096\n"
    "v_mfma_f32_16x16x32_bf16 v[192:195], v[108:111], v[148:151], v[192:195]\n"
    "ds_read_b128 v[88:91], v161 offset:0\n"
    "v_mfma_f32_16x16x32_bf16 v[196:199], v[104:107], v[152:155], v[196:199]\n"
    "ds_read_b128 v[92:95], v161 offset:2048\n"
    "v_mfma_f32_16x16x32_bf16 v[200:203], v[108:111], v[152:155], v[200:203]\n"
    "ds_read_b128 v[124:127], v163 offset:0\n"
    "v_mfma_f32_16x16x32_bf16 v[204:207], v[104:107], v[156:159], v[204:207]\n"
    "ds_read_b128 v[128:131], v163 offset:2048\n"
    "v_mfma_f32_16x16x32_bf16 v[208:211], v[108:111], v[156:159], v[208:211]\n"
    "ds_read_b128 v[132:135], v163 offset:4096\n"
    "s_waitcnt lgkmcnt(7)\n"
    "v_mfma_f32_16x16x32_bf16 v[188:191], v[80:83], v[112:115], v[188:191]\n"
    "ds_read_b128 v[96:99], v160 offset:8192\n"
    "v_mfma_f32_16x16x32_bf16 v[192:195], v[84:87], v[112:115], v[192:195]\n"
    "ds_read_b128 v[100:103], v160 offset:10240\n"
    "s_waitcnt lgkmcnt(8)\n"
    "v_mfma_f32_16x16x32_bf16 v[196:199], v[80:83], v[116:119], v[196:199]\n"
    "ds_read_b128 v[136:139], v162 offset:10240\n"
    "v_mfma_f32_16x16x32_bf16 v[200:203], v[84:87], v[116:119], v[200:203]\n"
    "ds_read_b128 v[140:143], v162 offset:12288\n"
    "s_waitcnt lgkmcnt(9)\n"
    "v_mfma_f32_16x16x32_bf16 v[204:207], v[80:83], v[120:123], v[204:207]\n"
    "ds_read_b128 v[144:147], v162 offset:14336\n"
    "v_mfma_f32_16x16x32_bf16 v[208:211], v[84:87], v[120:123], v[208:211]\n"
    "s_waitcnt lgkmcnt(5)\n"
    "v_mfma_f32_16x16x32_bf16 v[188:191], v[88:91], v[124:127], v[188:191]\n"
    "ds_read_b128 v[104:107], v161 offset:8192\n"
    "s_add_u32 s42, s42, 16384\n"
    "v_mfma_f32_16x16x32_bf16 v[192:195], v[92:95], v[124:127], v[192:195]\n"
    "ds_read_b128 v[108:111], v161 offset:10240\n"
    "s_lshr_b64 s[44:45], s[44:45], 4\n"
    "v_mfma_f32_16x16x32_bf16 v[196:199], v[88:91], v[128:131], v[196:199]\n"
    "ds_read_b128 v[148:151], v163 offset:10240\n"
    "s_cmp_eq_u32 s42, 49152\n"
    "s_cselect_b32 s42, 0, s42\n"
    "v_mfma_f32_16x16x32_bf16 v[200:203], v[92:95], v[128:131], v[200:203]\n"
    "ds_read_b128 v[152:155], v163 offset:12288\n"
    "s_and_b32 s43, s44, 15\n"
    "v_mfma_f32_16x16x32_bf16 v[204:207], v[88:91], v[132:135], v[204:207]\n"
    "ds_read_b128 v[156:159], v163 offset:14336\n"
    "s_mul_i32 s43, s43, 20480\n"
    "v_mfma_f32_16x16x32_bf16 v[208:211], v[92:95], v[132:135], v[208:211]\n"
    "s_waitcnt lgkmcnt(5)\n"
    "v_mfma_f32_16x16x32_bf16 v[188:191], v[96:99], v[136:139], v[188:191]\n"
    "v_add_u32 v160, s42, %0\n"
    "v_add_u32 v161, s42, %1\n"
    "v_mfma_f32_16x16x32_bf16 v[192:195], v[100:103], v[136:139], v[192:195]\n"
    "v_add_u32 v162, s43, %2\n"
    "v_add_u32 v163, s43, %3\n"
    "s_waitcnt lgkmcnt(0)\n"
    "s_barrier\n"
    "v_mfma_f32_16x16x32_bf16 v[196:199], v[96:99], v[140:143], v[196:199]\n"
    "ds_read_b128 v[80:83], v160 offset:0\n"
    "v_mfma_f32_16x16x32_bf16 v[200:203], v[100:103], v[140:143], v[200:203]\n"
    "ds_read_b128 v[84:87], v160 offset:2048\n"
    "v_mfma_f32_16x16x32_bf16 v[204:207], v[96:99], v[144:147], v[204:207]\n"
    "ds_read_b128 v[112:115], v162 offset:0\n"
    "v_mfma_f32_16x16x32_bf16 v[208:211], v[100:103], v[144:147], v[208:211]\n"
    "ds_read_b128 v[116:119], v162 offset:2048\n"
    "v_mfma_f32_16x16x32_bf16 v[188:191], v[104:107], v[148:151], v[188:191]\n"
    "ds_read_b128 v[120:123], v162 offset:4096\n"
    "v_mfma_f32_16x16x32_bf16 v[192:195], v[108:111], v[148:151], v[192:195]\n"
    "ds_read_b128 v[88:91], v161 offset:0\n"
    "v_mfma_f32_16x16x32_bf16 v[196:199], v[104:107], v[152:155], v[196:199]\n"
    "ds_read_b128 v[92:95], v161 offset:2048\n"
    "v_mfma_f32_16x16x32_bf16 v[200:203], v[108:111], v[152:155], v[200:203]\n"
    "ds_read_b128 v[124:127], v163 offset:0\n"
    "v_mfma_f32_16x16x32_bf16 v[204:207], v[104:107], v[156:159], v[204:207]\n"
    "ds_read_b128 v[128:131], v163 offset:2048\n"
    "v_mfma_f32_16x16x32_bf16 v[208:211], v[108:111], v[156:159], v[208:211]\n"
    "ds_read_b128 v[132:135], v163 offset:4096\n"
    "s_waitcnt lgkmcnt(7)\n"
    "v_mfma_f32_16x16x32_bf16 v[188:191], v[80:83], v[112:115], v[188:191]\n"
    "ds_read_b128 v[96:99], v160 offset:8192\n"
    "v_mfma_f32_16x16x32_bf16 v[192:195], v[84:87], v[112:115], v[192:195]\n"
    "ds_read_b128 v[100:103], v160 offset:10240\n"
    "s_waitcnt lgkmcnt(8)\n"
    "v_mfma_f32_16x16x32_bf16 v[196:199], v[80:83], v[116:119], v[196:199]\n"
    "ds_read_b128 v[136:139], v162 offset:10240\n"
    "v_mfma_f32_16x16x32_bf16 v[200:203], v[84:87], v[116:119], v[200:203]\n"
    "ds_read_b128 v[140:143], v162 offset:12288\n"
    "s_waitcnt lgkmcnt(9)\n"
    "v_mfma_f32_16x16x32_bf16 v[204:207], v[80:83], v[120:123], v[204:207]\n"
    "ds_read_b128 v[144:147], v162 offset:14336\n"
    "v_mfma_f32_16x16x32_bf16 v[208:211], v[84:87], v[120:123], v[208:211]\n"
    "s_waitcnt lgkmcnt(5)\n"
    "v_mfma_f32_16x16x32_bf16 v[188:191], v[88:91], v[124:127], v[188:191]\n"
    "ds_read_b128 v[104:107], v161 offset:8192\n"
    "s_add_u32 s42, s42, 16384\n"
    "v_mfma_f32_16x16x32_bf16 v[192:195], v[92:95], v[124:127], v[192:195]\n"
    "ds_read_b128 v[108:111], v161 offset:10240\n"
    "s_lshr_b64 s[44:45], s[44:45], 4\n"
    "v_mfma_f32_16x16x32_bf16 v[196:199], v[88:91], v[128:131], v[196:199]\n"
    "ds_read_b128 v[148:151], v163 offset:10240\n"
    "s_cmp_eq_u32 s42, 49152\n"
    "s_cselect_b32 s42, 0, s42\n"
    "v_mfma_f32_16x16x32_bf16 v[200:203], v[92:95], v[128:131], v[200:203]\n"
    "ds_read_b128 v[152:155], v163 offset:12288\n"
    "s_and_b32 s43, s44, 15\n"
    "v_mfma_f32_16x16x32_bf16 v[204:207], v[88:91], v[132:135], v[204:207]\n"
    "ds_read_b128 v[156:159], v163 offset:14336\n"
    "s_mul_i32 s43, s43, 20480\n"
    "v_mfma_f32_16x16x32_bf16 v[208:211], v[92:95], v[132:135], v[208:211]\n"
    "s_waitcnt lgkmcnt(5)\n"
    "v_mfma_f32_16x16x32_bf16 v[188:191], v[96:99], v[136:139], v[188:191]\n"
    "v_add_u32 v160, s42, %0\n"
    "v_add_u32 v161, s42, %1\n"
    "v_mfma_f32_16x16x32_bf16 v[192:195], v[100:103], v[136:139], v[192:195]\n"
    "v_add_u32 v162, s43, %2\n"
    "v_add_u32 v163, s43, %3\n"
    "s_waitcnt lgkmcnt(0)\n"
    "s_barrier\n"
    "v_mfma_f32_16x16x32_bf16 v[196:199], v[96:99], v[140:143], v[196:199]\n"
    "ds_read_b128 v[80:83], v160 offset:0\n"
    "v_mfma_f32_16x16x32_bf16 v[200:203], v[100:103], v[140:143], v[200:203]\n"
    "ds_read_b128 v[84:87], v160 offset:2048\n"
    "v_mfma_f32_16x16x32_bf16 v[204:207], v[96:99], v[144:147], v[204:207]\n"
    "ds_read_b128 v[112:115], v162 offset:0\n"
    "v_mfma_f32_16x16x32_bf16 v[208:211], v[100:103], v[144:147], v[208:211]\n"
    "ds_read_b128 v[116:119], v162 offset:2048\n"
    "v_mfma_f32_16x16x32_bf16 v[188:191], v[104:107], v[148:151], v[188:191]\n"
    "ds_read_b128 v[120:123], v162 offset:4096\n"
    "v_mfma_f32_16x16x32_bf16 v[192:195], v[108:111], v[148:151], v[192:195]\n"
    "ds_read_b128 v[88:91], v161 offset:0\n"
    "v_mfma_f32_16x16x32_bf16 v[196:199], v[104:107], v[152:155], v[196:199]\n"
    "ds_read_b128 v[92:95], v161 offset:2048\n"
    "v_mfma_f32_16x16x32_bf16 v[200:203], v[108:111], v[152:155], v[200:203]\n"
    "ds_read_b128 v[124:127], v163 offset:0\n"
    "v_mfma_f32_16x16x32_bf16 v[204:207], v[104:107], v[156:159], v[204:207]\n"
    "ds_read_b128 v[128:131], v163 offset:2048\n"
    "v_mfma_f32_16x16x32_bf16 v[208:211], v[108:111], v[156:159], v[208:211]\n"
    "ds_read_b128 v[132:135], v163 offset:4096\n"
    "s_waitcnt lgkmcnt(7)\n"
    "v_mfma_f32_16x16x32_bf16 v[188:191], v[80:83], v[112:115], v[188:191]\n"
    "ds_read_b128 v[96:99], v160 offset:8192\n"
    "v_mfma_f32_16x16x32_bf16 v[192:195], v[84:87], v[112:115], v[192:195]\n"
    "ds_read_b128 v[100:103], v160 offset:10240\n"
    "s_waitcnt lgkmcnt(8)\n"
    "v_mfma_f32_16x16x32_bf16 v[196:199], v[80:83], v[116:119], v[196:199]\n"
    "ds_read_b128 v[136:139], v162 offset:10240\n"
    "v_mfma_f32_16x16x32_bf16 v[200:203], v[84:87], v[116:119], v[200:203]\n"
    "ds_read_b128 v[140:143], v162 offset:12288\n"
    "s_waitcnt lgkmcnt(9)\n"
    "v_mfma_f32_16x16x32_bf16 v[204:207], v[80:83], v[120:123], v[204:207]\n"
    "ds_read_b128 v[144:147], v162 offset:14336\n"
    "v_mfma_f32_16x16x32_bf16 v[208:211], v[84:87], v[120:123], v[208:211]\n"
    "s_waitcnt lgkmcnt(5)\n"
    "v_mfma_f32_16x16x32_bf16 v[188:191], v[88:91], v[124:127], v[188:191]\n"
    "ds_read_b128 v[104:107], v161 offset:8192\n"
    "v_mfma_f32_16x16x32_bf16 v[192:195], v[92:95], v[124:127], v[192:195]\n"
    "ds_read_b128 v[108:111], v161 offset:10240\n"
    "v_mfma_f32_16x16x32_bf16 v[196:199], v[88:91], v[128:131], v[196:199]\n"
    "ds_read_b128 v[148:151], v163 offset:10240\n"
    "v_mfma_f32_16x16x32_bf16 v[200:203], v[92:95], v[128:131], v[200:203]\n"
    "ds_read_b128 v[152:155], v163 offset:12288\n"
    "v_mfma_f32_16x16x32_bf16 v[204:207], v[88:91], v[132:135], v[204:207]\n"
    "ds_read_b128 v[156:159], v163 offset:14336\n"
    "v_mfma_f32_16x16x32_bf16 v[208:211], v[92:95], v[132:135], v[208:211]\n"
    "s_waitcnt lgkmcnt(5)\n"
    "v_mfma_f32_16x16x32_bf16 v[188:191], v[96:99], v[136:139], v[188:191]\n"
    "v_mfma_f32_16x16x32_bf16 v[192:195], v[100:103], v[136:139], v[192:195]\n"
    "s_waitcnt lgkmcnt(0)\n"
    "s_barrier\n"
    "v_mfma_f32_16x16x32_bf16 v[196:199], v[96:99], v[140:143], v[196:199]\n"
    "v_mfma_f32_16x16x32_bf16 v[200:203], v[100:103], v[140:143], v[200:203]\n"
    "v_mfma_f32_16x16x32_bf16 v[204:207], v[96:99], v[144:147], v[204:207]\n"
    "v_mfma_f32_16x16x32_bf16 v[208:211], v[100:103], v[144:147], v[208:211]\n"
    "v_mfma_f32_16x16x32_bf16 v[188:191], v[104:107], v[148:151], v[188:191]\n"
    "v_mfma_f32_16x16x32_bf16 v[192:195], v[108:111], v[148:151], v[192:195]\n"
    "v_mfma_f32_16x16x32_bf16 v[196:199], v[104:107], v[152:155], v[196:199]\n"
    "v_mfma_f32_16x16x32_bf16 v[200:203], v[108:111], v[152:155], v[200:203]\n"
    "v_mfma_f32_16x16x32_bf16 v[204:207], v[104:107], v[156:159], v[204:207]\n"
    "v_mfma_f32_16x16x32_bf16 v[208:211], v[108:111], v[156:159], v[208:211]\n"
    "s_nop 2\n"
    "v_cvt_pk_bf16_f32 v188, v188, v189\n"
    "v_cvt_pk_bf16_f32 v189, v190, v191\n"
    "v_cvt_pk_bf16_f32 v190, v192, v193\n"
    "v_cvt_pk_bf16_f32 v191, v194, v195\n"
    "s_nop 0\n"
    "v_permlane16_swap_b32 v188, v190\n"
    "v_permlane16_swap_b32 v189, v191\n"
    "v_xor_b32 v188, %17, v188\n"
    "v_xor_b32 v189, %17, v189\n"
    "v_xor_b32 v190, %17, v190\n"
    "v_xor_b32 v191, %17, v191\n"
    "buffer_store_dwordx4 v[188:191], %4, %15, s47 offen offset:0 sc1\n"
    "v_cvt_pk_bf16_f32 v196, v196, v197\n"
    "v_cvt_pk_bf16_f32 v197, v198, v199\n"
    "v_cvt_pk_bf16_f32 v198, v200, v201\n"
    "v_cvt_pk_bf16_f32 v199, v202, v203\n"
    "s_nop 0\n"
    "v_permlane16_swap_b32 v196, v198\n"
    "v_permlane16_swap_b32 v197, v199\n"
    "v_xor_b32 v196, %17, v196\n"
    "v_xor_b32 v197, %17, v197\n"
    "v_xor_b32 v198, %17, v198\n"
    "v_xor_b32 v199, %17, v199\n"
    "buffer_store_dwordx4 v[196:199], %4, %15, s47 offen offset:2048 sc1\n"
    "v_cvt_pk_bf16_f32 v204, v204, v205\n"
    "v_cvt_pk_bf16_f32 v205, v206, v207\n"
    "v_cvt_pk_bf16_f32 v206, v208, v209\n"
    "v_cvt_pk_bf16_f32 v207, v210, v211\n"
    "s_nop 0\n"
    "v_permlane16_swap_b32 v204, v206\n"
    "v_permlane16_swap_b32 v205, v207\n"
    "v_xor_b32 v204, %17, v204\n"
    "v_xor_b32 v205, %17, v205\n"
    "v_xor_b32 v206, %17, v206\n"
    "v_xor_b32 v207, %17, v207\n"
    "buffer_store_dwordx4 v[204:207], %4, %15, s48 offen offset:0 sc1\n"
    "s_branch .Ljoin%=\n"
    ".Ltwo%=:\n"
    "s_mov_b64 s[44:45], %18\n"
    "s_mov_b32 s42, 0\n"
    "s_and_b32 s43, s44, 15\n"
    "s_mul_i32 s43, s43, 20480\n"
    "v_add_u32 v160, s42, %0\n"
    "v_add_u32 v161, s42, %1\n"
    "v_add_u32 v162, s43, %2\n"
    "v_add_u32 v163, s43, %3\n"
    "s_mov_b32 s40, 5\n"
    "s_barrier\n"
    "ds_read_b128 v[80:83], v160 offset:0\n"
    "ds_read_b128 v[84:87], v160 offset:2048\n"
    "ds_read_b128 v[112:115], v162 offset:0\n"
    "ds_read_b128 v[116:119], v162 offset:2048\n"
    "ds_read_b128 v[88:91], v161 offset:0\n"
    "ds_read_b128 v[92:95], v161 offset:2048\n"
    "ds_read_b128 v[124:127], v163 offset:0\n"
    "ds_read_b128 v[128:131], v163 offset:2048\n"
    ".Lloopb%=:\n"
    "s_waitcnt lgkmcnt(5)\n"
    "v_mfma_f32_16x16x32_bf16 v[164:167], v[80:83], v[112:115], v[164:167]\n"
    "ds_read_b128 v[96:99], v160 offset:8192\n"
    "v_mfma_f32_16x16x32_bf16 v[168:171], v[84:87], v[112:115], v[168:171]\n"
    "ds_read_b128 v[100:103], v160 offset:10240\n"
    "s_waitcnt lgkmcnt(6)\n"
    "v_mfma_f32_16x16x32_bf16 v[172:175], v[80:83], v[116:119], v[172:175]\n"
    "ds_read_b128 v[136:139], v162 offset:10240\n"
    "v_mfma_f32_16x16x32_bf16 v[176:179], v[84:87], v[116:119], v[176:179]\n"
    "ds_read_b128 v[140:143], v162 offset:12288\n"
    "s_waitcnt lgkmcnt(4)\n"
    "v_mfma_f32_16x16x32_bf16 v[164:167], v[88:91], v[124:127], v[164:167]\n"
    "ds_read_b128 v[104:107], v161 offset:8192\n"
    "s_add_u32 s42, s42, 16384\n"
    "v_mfma_f32_16x16x32_bf16 v[168:171], v[92:95], v[124:127], v[168:171]\n"
    "ds_read_b128 v[108:111], v161 offset:10240\n"
    "s_lshr_b64 s[44:45], s[44:45], 4\n"
    "v_mfma_f32_16x16x32_bf16 v[172:175], v[88:91], v[128:131], v[172:175]\n"
    "ds_read_b128 v[148:151], v163 offset:10240\n"
    "s_cmp_eq_u32 s42, 49152\n"
    "s_cselect_b32 s42, 0, s42\n"
    "v_mfma_f32_16x16x32_bf16 v[176:179], v[92:95], v[128:131], v[176:179]\n"
    "ds_read_b128 v[152:155], v163 offset:12288\n"
    "s_and_b32 s43, s44, 15\n"
    "s_waitcnt lgkmcnt(4)\n"
    "v_mfma_f32_16x16x32_bf16 v[164:167], v[96:99], v[136:139], v[164:167]\n"
    "s_mul_i32 s43, s43, 20480\n"
    "v_add_u32 v160, s42, %0\n"
    "v_add_u32 v161, s42, %1\n"
    "v_mfma_f32_16x16x32_bf16 v[168:171], v[100:103], v[136:139], v[168:171]\n"
    "v_add_u32 v162, s43, %2\n"
    "v_add_u32 v163, s43, %3\n"
    "s_waitcnt lgkmcnt(0)\n"
    "s_barrier\n"
    "v_mfma_f32_16x16x32_bf16 v[172:175], v[96:99], v[140:143], v[172:175]\n"
    "ds_read_b128 v[80:83], v160 offset:0\n"
    "ds_read_b128 v[84:87], v160 offset:2048\n"
    "v_mfma_f32_16x16x32_bf16 v[176:179], v[100:103], v[140:143], v[176:179]\n"
    "ds_read_b128 v[112:115], v162 offset:0\n"
    "ds_read_b128 v[116:119], v162 offset:2048\n"
    "v_mfma_f32_16x16x32_bf16 v[164:167], v[104:107], v[148:151], v[164:167]\n"
    "ds_read_b128 v[88:91], v161 offset:0\n"
    "v_mfma_f32_16x16x32_bf16 v[168:171], v[108:111], v[148:151], v[168:171]\n"
    "ds_read_b128 v[92:95], v161 offset:2048\n"
    "v_mfma_f32_16x16x32_bf16 v[172:175], v[104:107], v[152:155], v[172:175]\n"
    "ds_read_b128 v[124:127], v163 offset:0\n"
    "v_mfma_f32_16x16x32_bf16 v[176:179], v[108:111], v[152:155], v[176:179]\n"
    "ds_read_b128 v[128:131], v163 offset:2048\n"
    "s_sub_u32 s40, s40, 1\n"
    "s_cmp_lg_u32 s40, 0\n"
    "s_cbranch_scc1 .Lloopb%=\n"
    "s_waitcnt lgkmcnt(5)\n"
    "v_mfma_f32_16x16x32_bf16 v[188:191], v[80:83], v[112:115], v[188:191]\n"
    "ds_read_b128 v[96:99], v160 offset:8192\n"
    "v_cvt_pk_bf16_f32 v164, v164, v165\n"
    "v_cvt_pk_bf16_f32 v165, v166, v167\n"
    "v_cvt_pk_bf16_f32 v166, v168, v169\n"
    "v_mfma_f32_16x16x32_bf16 v[192:195], v[84:87], v[112:115], v[192:195]\n"
    "ds_read_b128 v[100:103], v160 offset:10240\n"
    "v_cvt_pk_bf16_f32 v167, v170, v171\n"
    "v_permlane16_swap_b32 v164, v166\n"
    "s_nop 0\n"
    "v_permlane16_swap_b32 v165, v167\n"
    "s_waitcnt lgkmcnt(6)\n"
    "v_mfma_f32_16x16x32_bf16 v[196:199], v[80:83], v[116:119], v[196:199]\n"
    "ds_read_b128 v[136:139], v162 offset:10240\n"
    "v_xor_b32 v164, %17, v164\n"
    "v_xor_b32 v165, %17, v165\n"
    "v_xor_b32 v166, %17, v166\n"
    "v_mfma_f32_16x16x32_bf16 v[200:203], v[84:87], v[116:119], v[200:203]\n"
    "ds_read_b128 v[140:143], v162 offset:12288\n"
    "v_xor_b32 v167, %17, v167\n"
    "buffer_store_dwordx4 v[164:167], %4, %15, 0 offen offset:0 sc1\n"
    "v_cvt_pk_bf16_f32 v172, v172, v173\n"
    "s_waitcnt lgkmcnt(4)\n"
    "v_mfma_f32_16x16x32_bf16 v[188:191], v[88:91], v[124:127], v[188:191]\n"
    "ds_read_b128 v[104:107], v161 offset:8192\n"
    "s_add_u32 s42, s42, 16384\n"
    "v_cvt_pk_bf16_f32 v173, v174, v175\n"
    "v_cvt_pk_bf16_f32 v174, v176, v177\n"
    "v_cvt_pk_bf16_f32 v175, v178, v179\n"
    "v_mfma_f32_16x16x32_bf16 v[192:195], v[92:95], v[124:127], v[192:195]\n"
    "ds_read_b128 v[108:111], v161 offset:10240\n"
    "s_lshr_b64 s[44:45], s[44:45], 4\n"
    "v_permlane16_swap_b32 v172, v174\n"
    "v_permlane16_swap_b32 v173, v175\n"
    "v_xor_b32 v172, %17, v172\n"
    "v_mfma_f32_16x16x32_bf16 v[196:199], v[88:91], v[128:131], v[196:199]\n"
    "ds_read_b128 v[148:151], v163 offset:10240\n"
    "s_cmp_eq_u32 s42, 49152\n"
    "s_cselect_b32 s42, 0, s42\n"
    "v_xor_b32 v173, %17, v173\n"
    "v_xor_b32 v174, %17, v174\n"
    "v_xor_b32 v175, %17, v175\n"
    "v_mfma_f32_16x16x32_bf16 v[200:203], v[92:95], v[128:131], v[200:203]\n"
    "ds_read_b128 v[152:155], v163 offset:12288\n"
    "s_and_b32 s43, s44, 15\n"
    "buffer_store_dwordx4 v[172:175], %4, %15, 0 offen offset:2048 sc1\n"
    "s_waitcnt lgkmcnt(4)\n"
    "v_mfma_f32_16x16x32_bf16 v[188:191], v[96:99], v[136:139], v[188:191]\n"
    "s_mul_i32 s43, s43, 20480\n"
    "v_add_u32 v160, s42, %0\n"
    "v_add_u32 v161, s42, %1\n"
    "v_mfma_f32_16x16x32_bf16 v[192:195], v[100:103], v[136:139], v[192:195]\n"
    "v_add_u32 v162, s43, %2\n"
    "v_add_u32 v163, s43, %3\n"
    "s_waitcnt lgkmcnt(0)\n"
    "s_barrier\n"
    "v_mfma_f32_16x16x32_bf16 v[196:199], v[96:99], v[140:143], v[196:199]\n"
    "ds_read_b128 v[80:83], v160 offset:0\n"
    "ds_read_b128 v[84:87], v160 offset:2048\n"
    "v_mfma_f32_16x16x32_bf16 v[200:203], v[100:103], v[140:143], v[200:203]\n"
    "ds_read_b128 v[112:115], v162 offset:0\n"
    "ds_read_b128 v[116:119], v162 offset:2048\n"
    "v_mfma_f32_16x16x32_bf16 v[188:191], v[104:107], v[148:151], v[188:191]\n"
    "ds_read_b128 v[88:91], v161 offset:0\n"
    "v_mfma_f32_16x16x32_bf16 v[192:195], v[108:111], v[148:151], v[192:195]\n"
    "ds_read_b128 v[92:95], v161 offset:2048\n"
    "v_mfma_f32_16x16x32_bf16 v[196:199], v[104:107], v[152:155], v[196:199]\n"
    "ds_read_b128 v[124:127], v163 offset:0\n"
    "v_mfma_f32_16x16x32_bf16 v[200:203], v[108:111], v[152:155], v[200:203]\n"
    "ds_read_b128 v[128:131], v163 offset:2048\n"
    "s_waitcnt lgkmcnt(5)\n"
    "v_mfma_f32_16x16x32_bf16 v[188:191], v[80:83], v[112:115], v[188:191]\n"
    "ds_read_b128 v[96:99], v160 offset:8192\n"
    "v_mfma_f32_16x16x32_bf16 v[192:195], v[84:87], v[112:115], v[192:195]\n"
    "ds_read_b128 v[100:103], v160 offset:10240\n"
    "s_waitcnt lgkmcnt(6)\n"
    "v_mfma_f32_16x16x32_bf16 v[196:199], v[80:83], v[116:119], v[196:199]\n"
    "ds_read_b128 v[136:139], v162 offset:10240\n"
    "v_mfma_f32_16x16x32_bf16 v[200:203], v[84:87], v[116:119], v[200:203]\n"
    "ds_read_b128 v[140:143], v162 offset:12288\n"
    "s_waitcnt lgkmcnt(4)\n"
    "v_mfma_f32_16x16x32_bf16 v[188:191], v[88:91], v[124:127], v[188:191]\n"
    "ds_read_b128 v[104:107], v161 offset:8192\n"
    "s_add_u32 s42, s42, 16384\n"
    "v_mfma_f32_16x16x32_bf16 v[192:195], v[92:95], v[124:127], v[192:195]\n"
    "ds_read_b128 v[108:111], v161 offset:10240\n"
    "s_lshr_b64 s[44:45], s[44:45], 4\n"
    "v_mfma_f32_16x16x32_bf16 v[196:199], v[88:91], v[128:131], v[196:199]\n"
    "ds_read_b128 v[148:151], v163 offset:10240\n"
    "s_cmp_eq_u32 s42, 49152\n"
    "s_cselect_b32 s42, 0, s42\n"
    "v_mfma_f32_16x16x32_bf16 v[200:203], v[92:95], v[128:131], v[200:203]\n"
    "ds_read_b128 v[152:155], v163 offset:12288\n"
    "s_and_b32 s43, s44, 15\n"
    "s_waitcnt lgkmcnt(4)\n"
    "v_mfma_f32_16x16x32_bf16 v[188:191], v[96:99], v[136:139], v[188:191]\n"
    "s_mul_i32 s43, s43, 20480\n"
    "v_add_u32 v160, s42, %0\n"
    "v_add_u32 v161, s42, %1\n"
    "v_mfma_f32_16x16x32_bf16 v[192:195], v[100:103], v[136:139], v[192:195]\n"
    "v_add_u32 v162, s43, %2\n"
    "v_add_u32 v163, s43, %3\n"
    "s_waitcnt lgkmcnt(0)\n"
    "s_waitcnt vmcnt(0)\n"
    "s_barrier\n"
    "v_mfma_f32_16x16x32_bf16 v[196:199], v[96:99], v[140:143], v[196:199]\n"
    "ds_read_b128 v[80:83], v160 offset:0\n"
    "ds_read_b128 v[84:87], v160 offset:2048\n"
    "v_mfma_f32_16x16x32_bf16 v[200:203], v[100:103], v[140:143], v[200:203]\n"
    "ds_read_b128 v[112:115], v162 offset:0\n"
    "ds_read_b128 v[116:119], v162 offset:2048\n"
    "v_mfma_f32_16x16x32_bf16 v[188:191], v[104:107], v[148:151], v[188:191]\n"
    "ds_read_b128 v[88:91], v161 offset:0\n"
    "v_mfma_f32_16x16x32_bf16 v[192:195], v[108:111], v[148:151], v[192:195]\n"
    "ds_read_b128 v[92:95], v161 offset:2048\n"
    "v_mfma_f32_16x16x32_bf16 v[196:199], v[104:107], v[152:155], v[196:199]\n"
    "ds_read_b128 v[124:127], v163 offset:0\n"
    "v_mfma_f32_16x16x32_bf16 v[200:203], v[108:111], v[152:155], v[200:203]\n"
    "ds_read_b128 v[128:131], v163 offset:2048\n"
    "s_waitcnt lgkmcnt(5)\n"
    "v_mfma_f32_16x16x32_bf16 v[188:191], v[80:83], v[112:115], v[188:191]\n"
    "ds_read_b128 v[96:99], v160 offset:8192\n"
    "v_mfma_f32_16x16x32_bf16 v[192:195], v[84:87], v[112:115], v[192:195]\n"
    "ds_read_b128 v[100:103], v160 offset:10240\n"
    "s_waitcnt lgkmcnt(6)\n"
    "v_mfma_f32_16x16x32_bf16 v[196:199], v[80:83], v[116:119], v[196:199]\n"
    "ds_read_b128 v[136:139], v162 offset:10240\n"
    "v_mfma_f32_16x16x32_bf16 v[200:203], v[84:87], v[116:119], v[200:203]\n"
    "ds_read_b128 v[140:143], v162 offset:12288\n"
    "s_waitcnt lgkmcnt(4)\n"
    "v_mfma_f32_16x16x32_bf16 v[188:191], v[88:91], v[124:127], v[188:191]\n"
    "ds_read_b128 v[104:107], v161 offset:8192\n"
    "s_add_u32 s42, s42, 16384\n"
    "v_mfma_f32_16x16x32_bf16 v[192:195], v[92:95], v[124:127], v[192:195]\n"
    "ds_read_b128 v[108:111], v161 offset:10240\n"
    "s_lshr_b64 s[44:45], s[44:45], 4\n"
    "v_mfma_f32_16x16x32_bf16 v[196:199], v[88:91], v[128:131], v[196:199]\n"
    "ds_read_b128 v[148:151], v163 offset:10240\n"
    "s_cmp_eq_u32 s42, 49152\n"
    "s_cselect_b32 s42, 0, s42\n"
    "v_mfma_f32_16x16x32_bf16 v[200:203], v[92:95], v[128:131], v[200:203]\n"
    "ds_read_b128 v[152:155], v163 offset:12288\n"
    "s_and_b32 s43, s44, 15\n"
    "s_waitcnt lgkmcnt(4)\n"
    "v_mfma_f32_16x16x32_bf16 v[188:191], v[96:99], v[136:139], v[188:191]\n"
    "s_mul_i32 s43, s43, 20480\n"
    "v_add_u32 v160, s42, %0\n"
    "v_add_u32 v161, s42, %1\n"
    "v_mfma_f32_16x16x32_bf16 v[192:195], v[100:103], v[136:139], v[192:195]\n"
    "v_add_u32 v162, s43, %2\n"
    "v_add_u32 v163, s43, %3\n"
    "s_waitcnt lgkmcnt(0)\n"
    "s_barrier\n"
    "v_mfma_f32_16x16x32_bf16 v[196:199], v[96:99], v[140:143], v[196:199]\n"
    "ds_read_b128 v[80:83], v160 offset:0\n"
    "ds_read_b128 v[84:87], v160 offset:2048\n"
    "v_mfma_f32_16x16x32_bf16 v[200:203], v[100:103], v[140:143], v[200:203]\n"
    "ds_read_b128 v[112:115], v162 offset:0\n"
    "ds_read_b128 v[116:119], v162 offset:2048\n"
    "v_mfma_f32_16x16x32_bf16 v[188:191], v[104:107], v[148:151], v[188:191]\n"
    "ds_read_b128 v[88:91], v161 offset:0\n"
    "v_mfma_f32_16x16x32_bf16 v[192:195], v[108:111], v[148:151], v[192:195]\n"
    "ds_read_b128 v[92:95], v161 offset:2048\n"
    "v_mfma_f32_16x16x32_bf16 v[196:199], v[104:107], v[152:155], v[196:199]\n"
    "ds_read_b128 v[124:127], v163 offset:0\n"
    "v_mfma_f32_16x16x32_bf16 v[200:203], v[108:111], v[152:155], v[200:203]\n"
    "ds_read_b128 v[128:131], v163 offset:2048\n"
    "s_waitcnt lgkmcnt(5)\n"
    "v_mfma_f32_16x16x32_bf16 v[188:191], v[80:83], v[112:115], v[188:191]\n"
    "ds_read_b128 v[96:99], v160 offset:8192\n"
    "v_mfma_f32_16x16x32_bf16 v[192:195], v[84:87], v[112:115], v[192:195]\n"
    "ds_read_b128 v[100:103], v160 offset:10240\n"
    "s_waitcnt lgkmcnt(6)\n"
    "v_mfma_f32_16x16x32_bf16 v[196:199], v[80:83], v[116:119], v[196:199]\n"
    "ds_read_b128 v[136:139], v162 offset:10240\n"
    "v_mfma_f32_16x16x32_bf16 v[200:203], v[84:87], v[116:119], v[200:203]\n"
    "ds_read_b128 v[140:143], v162 offset:12288\n"
    "s_waitcnt lgkmcnt(4)\n"
    "v_mfma_f32_16x16x32_bf16 v[188:191], v[88:91], v[124:127], v[188:191]\n"
    "ds_read_b128 v[104:107], v161 offset:8192\n"
    "s_add_u32 s42, s42, 16384\n"
    "v_mfma_f32_16x16x32_bf16 v[192:195], v[92:95], v[124:127], v[192:195]\n"
    "ds_read_b128 v[108:111], v161 offset:10240\n"
    "s_lshr_b64 s[44:45], s[44:45], 4\n"
    "v_mfma_f32_16x16x32_bf16 v[196:199], v[88:91], v[128:131], v[196:199]\n"
    "ds_read_b128 v[148:151], v163 offset:10240\n"
    "s_cmp_eq_u32 s42, 49152\n"
    "s_cselect_b32 s42, 0, s42\n"
    "v_mfma_f32_16x16x32_bf16 v[200:203], v[92:95], v[128:131], v[200:203]\n"
    "ds_read_b128 v[152:155], v163 offset:12288\n"
    "s_and_b32 s43, s44, 15\n"
    "s_waitcnt lgkmcnt(4)\n"
    "v_mfma_f32_16x16x32_bf16 v[188:191], v[96:99], v[136:139], v[188:191]\n"
    "s_mul_i32 s43, s43, 20480\n"
    "v_add_u32 v160, s42, %0\n"
    "v_add_u32 v161, s42, %1\n"
    "v_mfma_f32_16x16x32_bf16 v[192:195], v[100:103], v[136:139], v[192:195]\n"
    "v_add_u32 v162, s43, %2\n"
    "v_add_u32 v163, s43, %3\n"
    "s_waitcnt lgkmcnt(0)\n"
    "s_barrier\n"
    "v_mfma_f32_16x16x32_bf16 v[196:199], v[96:99], v[140:143], v[196:199]\n"
    "ds_read_b128 v[80:83], v160 offset:0\n"
    "ds_read_b128 v[84:87], v160 offset:2048\n"
    "v_mfma_f32_16x16x32_bf16 v[200:203], v[100:103], v[140:143], v[200:203]\n"
    "ds_read_b128 v[112:115], v162 offset:0\n"
    "ds_read_b128 v[116:119], v162 offset:2048\n"
    "v_mfma_f32_16x16x32_bf16 v[188:191], v[104:107], v[148:151], v[188:191]\n"
    "ds_read_b128 v[88:91], v161 offset:0\n"
    "v_mfma_f32_16x16x32_bf16 v[192:195], v[108:111], v[148:151], v[192:195]\n"
    "ds_read_b128 v[92:95], v161 offset:2048\n"
    "v_mfma_f32_16x16x32_bf16 v[196:199], v[104:107], v[152:155], v[196:199]\n"
    "ds_read_b128 v[124:127], v163 offset:0\n"
    "v_mfma_f32_16x16x32_bf16 v[200:203], v[108:111], v[152:155], v[200:203]\n"
    "ds_read_b128 v[128:131], v163 offset:2048\n"
    "s_waitcnt lgkmcnt(5)\n"
    "v_mfma_f32_16x16x32_bf16 v[188:191], v[80:83], v[112:115], v[188:191]\n"
    "ds_read_b128 v[96:99], v160 offset:8192\n"
    "v_mfma_f32_16x16x32_bf16 v[192:195], v[84:87], v[112:115], v[192:195]\n"
    "ds_read_b128 v[100:103], v160 offset:10240\n"
    "s_waitcnt lgkmcnt(6)\n"
    "v_mfma_f32_16x16x32_bf16 v[196:199], v[80:83], v[116:119], v[196:199]\n"
    "ds_read_b128 v[136:139], v162 offset:10240\n"
    "v_mfma_f32_16x16x32_bf16 v[200:203], v[84:87], v[116:119], v[200:203]\n"
    "ds_read_b128 v[140:143], v162 offset:12288\n"
    "s_waitcnt lgkmcnt(4)\n"
    "v_mfma_f32_16x16x32_bf16 v[188:191], v[88:91], v[124:127], v[188:191]\n"
    "ds_read_b128 v[104:107], v161 offset:8192\n"
    "v_mfma_f32_16x16x32_bf16 v[192:195], v[92:95], v[124:127], v[192:195]\n"
    "ds_read_b128 v[108:111], v161 offset:10240\n"
    "v_mfma_f32_16x16x32_bf16 v[196:199], v[88:91], v[128:131], v[196:199]\n"
    "ds_read_b128 v[148:151], v163 offset:10240\n"
    "v_mfma_f32_16x16x32_bf16 v[200:203], v[92:95], v[128:131], v[200:203]\n"
    "ds_read_b128 v[152:155], v163 offset:12288\n"
    "s_waitcnt lgkmcnt(4)\n"
    "v_mfma_f32_16x16x32_bf16 v[188:191], v[96:99], v[136:139], v[188:191]\n"
    "v_mfma_f32_16x16x32_bf16 v[192:195], v[100:103], v[136:139], v[192:195]\n"
    "s_waitcnt lgkmcnt(0)\n"
    "s_barrier\n"
    "v_mfma_f32_16x16x32_bf16 v[196:199], v[96:99], v[140:143], v[196:199]\n"
    "v_mfma_f32_16x16x32_bf16 v[200:203], v[100:103], v[140:143], v[200:203]\n"
    "v_mfma_f32_16x16x32_bf16 v[188:191], v[104:107], v[148:151], v[188:191]\n"
    "v_mfma_f32_16x16x32_bf16 v[192:195], v[108:111], v[148:151], v[192:195]\n"
    "v_mfma_f32_16x16x32_bf16 v[196:199], v[104:107], v[152:155], v[196:199]\n"
    "v_mfma_f32_16x16x32_bf16 v[200:203], v[108:111], v[152:155], v[200:203]\n"
    "s_nop 4\n"
    "v_cvt_pk_bf16_f32 v188, v188, v189\n"
    "v_cvt_pk_bf16_f32 v189, v190, v191\n"
    "v_cvt_pk_bf16_f32 v190, v192, v193\n"
    "v_cvt_pk_bf16_f32 v191, v194, v195\n"
    "s_nop 0\n"
    "v_permlane16_swap_b32 v188, v190\n"
    "v_permlane16_swap_b32 v189, v191\n"
    "v_xor_b32 v188, %17, v188\n"
    "v_xor_b32 v189, %17, v189\n"
    "v_xor_b32 v190, %17, v190\n"
    "v_xor_b32 v191, %17, v191\n"
    "buffer_store_dwordx4 v[188:191], %4, %15, s47 offen offset:0 sc1\n"
    "v_cvt_pk_bf16_f32 v196, v196, v197\n"
    "v_cvt_pk_bf16_f32 v197, v198, v199\n"
    "v_cvt_pk_bf16_f32 v198, v200, v201\n"
    "v_cvt_pk_bf16_f32 v199, v202, v203\n"
    "s_nop 0\n"
    "v_permlane16_swap_b32 v196, v198\n"
    "v_permlane16_swap_b32 v197, v199\n"
    "v_xor_b32 v196, %17, v196\n"
    "v_xor_b32 v197, %17, v197\n"
    "v_xor_b32 v198, %17, v198\n"
    "v_xor_b32 v199, %17, v199\n"
    "buffer_store_dwordx4 v[196:199], %4, %15, s47 offen offset:2048 sc1\n"
    ".Ljoin%=:\n"
    "s_barrier\n"
    "ds_read_b128 v[80:83], %5 offset:16384\n"
    "ds_read_b128 v[84:87], %5 offset:18432\n"
    "ds_read_b128 v[88:91], %5 offset:20480\n"
    "ds_read_b128 v[92:95], %5 offset:22528\n"
    "ds_read_b128 v[96:99], %7 offset:0\n"
    "ds_read_b128 v[100:103], %7 offset:2048\n"
    "ds_read_b128 v[104:107], %7 offset:4096\n"
    "ds_read_b128 v[108:111], %7 offset:6144\n"
    "ds_read_b128 v[112:115], %7 offset:8192\n"
    "s_mov_b32 s40, 3\n"
    ".Lloopc%=:\n"
    "s_waitcnt lgkmcnt(4)\n"
    "v_mfma_f32_16x16x32_bf16 v[0:3], v[96:99], v[80:83], v[0:3]\n"
    "ds_read_b128 v[116:119], %6 offset:16384\n"
    "v_mfma_f32_16x16x32_bf16 v[20:23], v[96:99], v[84:87], v[20:23]\n"
    "v_mfma_f32_16x16x32_bf16 v[40:43], v[96:99], v[88:91], v[40:43]\n"
    "ds_read_b128 v[120:123], %6 offset:18432\n"
    "v_mfma_f32_16x16x32_bf16 v[60:63], v[96:99], v[92:95], v[60:63]\n"
    "s_waitcnt lgkmcnt(5)\n"
    "v_mfma_f32_16x16x32_bf16 v[4:7], v[100:103], v[80:83], v[4:7]\n"
    "ds_read_b128 v[124:127], %6 offset:20480\n"
    "v_mfma_f32_16x16x32_bf16 v[24:27], v[100:103], v[84:87], v[24:27]\n"
    "v_mfma_f32_16x16x32_bf16 v[44:47], v[100:103], v[88:91], v[44:47]\n"
    "ds_read_b128 v[128:131], %6 offset:22528\n"
    "v_mfma_f32_16x16x32_bf16 v[64:67], v[100:103], v[92:95], v[64:67]\n"
    "s_waitcnt lgkmcnt(6)\n"
    "v_mfma_f32_16x16x32_bf16 v[8:11], v[104:107], v[80:83], v[8:11]\n"
    "ds_read_b128 v[132:135], %7 offset:1024\n"
    "v_mfma_f32_16x16x32_bf16 v[28:31], v[104:107], v[84:87], v[28:31]\n"
    "v_mfma_f32_16x16x32_bf16 v[48:51], v[104:107], v[88:91], v[48:51]\n"
    "ds_read_b128 v[136:139], %7 offset:3072\n"
    "v_mfma_f32_16x16x32_bf16 v[68:71], v[104:107], v[92:95], v[68:71]\n"
    "s_waitcnt lgkmcnt(7)\n"
    "v_mfma_f32_16x16x32_bf16 v[12:15], v[108:111], v[80:83], v[12:15]\n"
    "ds_read_b128 v[140:143], %7 offset:5120\n"
    "v_mfma_f32_16x16x32_bf16 v[32:35], v[108:111], v[84:87], v[32:35]\n"
    "v_mfma_f32_16x16x32_bf16 v[52:55], v[108:111], v[88:91], v[52:55]\n"
    "ds_read_b128 v[144:147], %7 offset:7168\n"
    "v_mfma_f32_16x16x32_bf16 v[72:75], v[108:111], v[92:95], v[72:75]\n"
    "s_waitcnt lgkmcnt(8)\n"
    "v_mfma_f32_16x16x32_bf16 v[16:19], v[112:115], v[80:83], v[16:19]\n"
    "ds_read_b128 v[148:151], %7 offset:9216\n"
    "v_mfma_f32_16x16x32_bf16 v[36:39], v[112:115], v[84:87], v[36:39]\n"
    "v_mfma_f32_16x16x32_bf16 v[56:59], v[112:115], v[88:91], v[56:59]\n"
    "v_mfma_f32_16x16x32_bf16 v[76:79], v[112:115], v[92:95], v[76:79]\n"
    "s_waitcnt lgkmcnt(4)\n"
    "v_mfma_f32_16x16x32_bf16 v[0:3], v[132:135], v[116:119], v[0:3]\n"
    "v_mfma_f32_16x16x32_bf16 v[20:23], v[132:135], v[120:123], v[20:23]\n"
    "s_waitcnt lgkmcnt(0)\n"
    "v_mfma_f32_16x16x32_bf16 v[40:43], v[132:135], v[124:127], v[40:43]\n"
    "s_waitcnt vmcnt(0)\n"
    "s_barrier\n"
    "v_mfma_f32_16x16x32_bf16 v[60:63], v[132:135], v[128:131], v[60:63]\n"
    "ds_read_b128 v[80:83], %5 offset:32768\n"
    "v_mfma_f32_16x16x32_bf16 v[4:7], v[136:139], v[116:119], v[4:7]\n"
    "v_mfma_f32_16x16x32_bf16 v[24:27], v[136:139], v[120:123], v[24:27]\n"
    "ds_read_b128 v[84:87], %5 offset:34816\n"
    "v_mfma_f32_16x16x32_bf16 v[44:47], v[136:139], v[124:127], v[44:47]\n"
    "v_mfma_f32_16x16x32_bf16 v[64:67], v[136:139], v[128:131], v[64:67]\n"
    "ds_read_b128 v[88:91], %5 offset:36864\n"
    "v_mfma_f32_16x16x32_bf16 v[8:11], v[140:143], v[116:119], v[8:11]\n"
    "v_mfma_f32_16x16x32_bf16 v[28:31], v[140:143], v[120:123], v[28:31]\n"
    "ds_read_b128 v[92:95], %5 offset:38912\n"
    "v_mfma_f32_16x16x32_bf16 v[48:51], v[140:143], v[124:127], v[48:51]\n"
    "v_mfma_f32_16x16x32_bf16 v[68:71], v[140:143], v[128:131], v[68:71]\n"
    "ds_read_b128 v[96:99], %7 offset:20480\n"
    "v_mfma_f32_16x16x32_bf16 v[12:15], v[144:147], v[116:119], v[12:15]\n"
    "v_mfma_f32_16x16x32_bf16 v[32:35], v[144:147], v[120:123], v[32:35]\n"
    "ds_read_b128 v[100:103], %7 offset:22528\n"
    "v_mfma_f32_16x16x32_bf16 v[52:55], v[144:147], v[124:127], v[52:55]\n"
    "v_mfma_f32_16x16x32_bf16 v[72:75], v[144:147], v[128:131], v[72:75]\n"
    "ds_read_b128 v[104:107], %7 offset:24576\n"
    "v_mfma_f32_16x16x32_bf16 v[16:19], v[148:151], v[116:119], v[16:19]\n"
    "v_mfma_f32_16x16x32_bf16 v[36:39], v[148:151], v[120:123], v[36:39]\n"
    "ds_read_b128 v[108:111], %7 offset:26624\n"
    "v_mfma_f32_16x16x32_bf16 v[56:59], v[148:151], v[124:127], v[56:59]\n"
    "v_mfma_f32_16x16x32_bf16 v[76:79], v[148:151], v[128:131], v[76:79]\n"
    "ds_read_b128 v[112:115], %7 offset:28672\n"
    "s_waitcnt lgkmcnt(4)\n"
    "v_mfma_f32_16x16x32_bf16 v[0:3], v[96:99], v[80:83], v[0:3]\n"
    "ds_read_b128 v[116:119], %6 offset:32768\n"
    "v_mfma_f32_16x16x32_bf16 v[20:23], v[96:99], v[84:87], v[20:23]\n"
    "v_mfma_f32_16x16x32_bf16 v[40:43], v[96:99], v[88:91], v[40:43]\n"
    "ds_read_b128 v[120:123], %6 offset:34816\n"
    "v_mfma_f32_16x16x32_bf16 v[60:63], v[96:99], v[92:95], v[60:63]\n"
    "s_waitcnt lgkmcnt(5)\n"
    "v_mfma_f32_16x16x32_bf16 v[4:7], v[100:103], v[80:83], v[4:7]\n"
    "ds_read_b128 v[124:127], %6 offset:36864\n"
    "v_mfma_f32_16x16x32_bf16 v[24:27], v[100:103], v[84:87], v[24:27]\n"
    "v_mfma_f32_16x16x32_bf16 v[44:47], v[100:103], v[88:91], v[44:47]\n"
    "ds_read_b128 v[128:131], %6 offset:38912\n"
    "v_mfma_f32_16x16x32_bf16 v[64:67], v[100:103], v[92:95], v[64:67]\n"
    "s_waitcnt lgkmcnt(6)\n"
    "v_mfma_f32_16x16x32_bf16 v[8:11], v[104:107], v[80:83], v[8:11]\n"
    "ds_read_b128 v[132:135], %7 offset:21504\n"
    "v_mfma_f32_16x16x32_bf16 v[28:31], v[104:107], v[84:87], v[28:31]\n"
    "v_mfma_f32_16x16x32_bf16 v[48:51], v[104:107], v[88:91], v[48:51]\n"
    "ds_read_b128 v[136:139], %7 offset:23552\n"
    "v_mfma_f32_16x16x32_bf16 v[68:71], v[104:107], v[92:95], v[68:71]\n"
    "s_waitcnt lgkmcnt(7)\n"
    "v_mfma_f32_16x16x32_bf16 v[12:15], v[108:111], v[80:83], v[12:15]\n"
    "ds_read_b128 v[140:143], %7 offset:25600\n"
    "v_mfma_f32_16x16x32_bf16 v[32:35], v[108:111], v[84:87], v[32:35]\n"
    "v_mfma_f32_16x16x32_bf16 v[52:55], v[108:111], v[88:91], v[52:55]\n"
    "ds_read_b128 v[144:147], %7 offset:27648\n"
    "v_mfma_f32_16x16x32_bf16 v[72:75], v[108:111], v[92:95], v[72:75]\n"
    "s_waitcnt lgkmcnt(8)\n"
    "v_mfma_f32_16x16x32_bf16 v[16:19], v[112:115], v[80:83], v[16:19]\n"
    "ds_read_b128 v[148:151], %7 offset:29696\n"
    "v_mfma_f32_16x16x32_bf16 v[36:39], v[112:115], v[84:87], v[36:39]\n"
    "v_mfma_f32_16x16x32_bf16 v[56:59], v[112:115], v[88:91], v[56:59]\n"
    "v_mfma_f32_16x16x32_bf16 v[76:79], v[112:115], v[92:95], v[76:79]\n"
    "s_waitcnt lgkmcnt(4)\n"
    "v_mfma_f32_16x16x32_bf16 v[0:3], v[132:135], v[116:119], v[0:3]\n"
    "v_mfma_f32_16x16x32_bf16 v[20:23], v[132:135], v[120:123], v[20:23]\n"
    "s_waitcnt lgkmcnt(0)\n"
    "v_mfma_f32_16x16x32_bf16 v[40:43], v[132:135], v[124:127], v[40:43]\n"
    "s_barrier\n"
    "v_mfma_f32_16x16x32_bf16 v[60:63], v[132:135], v[128:131], v[60:63]\n"
    "ds_read_b128 v[80:83], %5 offset:0\n"
    "v_mfma_f32_16x16x32_bf16 v[4:7], v[136:139], v[116:119], v[4:7]\n"
    "v_mfma_f32_16x16x32_bf16 v[24:27], v[136:139], v[120:123], v[24:27]\n"
    "ds_read_b128 v[84:87], %5 offset:2048\n"
    "v_mfma_f32_16x16x32_bf16 v[44:47], v[136:139], v[124:127], v[44:47]\n"
    "v_mfma_f32_16x16x32_bf16 v[64:67], v[136:139], v[128:131], v[64:67]\n"
    "ds_read_b128 v[88:91], %5 offset:4096\n"
    "v_mfma_f32_16x16x32_bf16 v[8:11], v[140:143], v[116:119], v[8:11]\n"
    "v_mfma_f32_16x16x32_bf16 v[28:31], v[140:143], v[120:123], v[28:31]\n"
    "ds_read_b128 v[92:95], %5 offset:6144\n"
    "v_mfma_f32_16x16x32_bf16 v[48:51], v[140:143], v[124:127], v[48:51]\n"
    "v_mfma_f32_16x16x32_bf16 v[68:71], v[140:143], v[128:131], v[68:71]\n"
    "ds_read_b128 v[96:99], %7 offset:40960\n"
    "v_mfma_f32_16x16x32_bf16 v[12:15], v[144:147], v[116:119], v[12:15]\n"
    "v_mfma_f32_16x16x32_bf16 v[32:35], v[144:147], v[120:123], v[32:35]\n"
    "ds_read_b128 v[100:103], %7 offset:43008\n"
    "v_mfma_f32_16x16x32_bf16 v[52:55], v[144:147], v[124:127], v[52:55]\n"
    "v_mfma_f32_16x16x32_bf16 v[72:75], v[144:147], v[128:131], v[72:75]\n"
    "ds_read_b128 v[104:107], %7 offset:45056\n"
    "v_mfma_f32_16x16x32_bf16 v[16:19], v[148:151], v[116:119], v[16:19]\n"
    "v_mfma_f32_16x16x32_bf16 v[36:39], v[148:151], v[120:123], v[36:39]\n"
    "ds_read_b128 v[108:111], %7 offset:47104\n"
    "v_mfma_f32_16x16x32_bf16 v[56:59], v[148:151], v[124:127], v[56:59]\n"
    "v_mfma_f32_16x16x32_bf16 v[76:79], v[148:151], v[128:131], v[76:79]\n"
    "ds_read_b128 v[112:115], %7 offset:49152\n"
    "s_waitcnt lgkmcnt(4)\n"
    "v_mfma_f32_16x16x32_bf16 v[0:3], v[96:99], v[80:83], v[0:3]\n"
    "ds_read_b128 v[116:119], %6 offset:0\n"
    "v_mfma_f32_16x16x32_bf16 v[20:23], v[96:99], v[84:87], v[20:23]\n"
    "v_mfma_f32_16x16x32_bf16 v[40:43], v[96:99], v[88:91], v[40:43]\n"
    "ds_read_b128 v[120:123], %6 offset:2048\n"
    "v_mfma_f32_16x16x32_bf16 v[60:63], v[96:99], v[92:95], v[60:63]\n"
    "s_waitcnt lgkmcnt(5)\n"
    "v_mfma_f32_16x16x32_bf16 v[4:7], v[100:103], v[80:83], v[4:7]\n"
    "ds_read_b128 v[124:127], %6 offset:4096\n"
    "v_mfma_f32_16x16x32_bf16 v[24:27], v[100:103], v[84:87], v[24:27]\n"
    "v_mfma_f32_16x16x32_bf16 v[44:47], v[100:103], v[88:91], v[44:47]\n"
    "ds_read_b128 v[128:131], %6 offset:6144\n"
    "v_mfma_f32_16x16x32_bf16 v[64:67], v[100:103], v[92:95], v[64:67]\n"
    "s_waitcnt lgkmcnt(6)\n"
    "v_mfma_f32_16x16x32_bf16 v[8:11], v[104:107], v[80:83], v[8:11]\n"
    "ds_read_b128 v[132:135], %7 offset:41984\n"
    "v_mfma_f32_16x16x32_bf16 v[28:31], v[104:107], v[84:87], v[28:31]\n"
    "v_mfma_f32_16x16x32_bf16 v[48:51], v[104:107], v[88:91], v[48:51]\n"
    "ds_read_b128 v[136:139], %7 offset:44032\n"
    "v_mfma_f32_16x16x32_bf16 v[68:71], v[104:107], v[92:95], v[68:71]\n"
    "s_waitcnt lgkmcnt(7)\n"
    "v_mfma_f32_16x16x32_bf16 v[12:15], v[108:111], v[80:83], v[12:15]\n"
    "ds_read_b128 v[140:143], %7 offset:46080\n"
    "v_mfma_f32_16x16x32_bf16 v[32:35], v[108:111], v[84:87], v[32:35]\n"
    "v_mfma_f32_16x16x32_bf16 v[52:55], v[108:111], v[88:91], v[52:55]\n"
    "ds_read_b128 v[144:147], %7 offset:48128\n"
    "v_mfma_f32_16x16x32_bf16 v[72:75], v[108:111], v[92:95], v[72:75]\n"
    "s_waitcnt lgkmcnt(8)\n"
    "v_mfma_f32_16x16x32_bf16 v[16:19], v[112:115], v[80:83], v[16:19]\n"
    "ds_read_b128 v[148:151], %7 offset:50176\n"
    "v_mfma_f32_16x16x32_bf16 v[36:39], v[112:115], v[84:87], v[36:39]\n"
    "v_mfma_f32_16x16x32_bf16 v[56:59], v[112:115], v[88:91], v[56:59]\n"
    "v_mfma_f32_16x16x32_bf16 v[76:79], v[112:115], v[92:95], v[76:79]\n"
    "s_waitcnt lgkmcnt(4)\n"
    "v_mfma_f32_16x16x32_bf16 v[0:3], v[132:135], v[116:119], v[0:3]\n"
    "v_mfma_f32_16x16x32_bf16 v[20:23], v[132:135], v[120:123], v[20:23]\n"
    "s_waitcnt lgkmcnt(0)\n"
    "v_mfma_f32_16x16x32_bf16 v[40:43], v[132:135], v[124:127], v[40:43]\n"
    "s_barrier\n"
    "v_mfma_f32_16x16x32_bf16 v[60:63], v[132:135], v[128:131], v[60:63]\n"
    "ds_read_b128 v[80:83], %5 offset:49152\n"
    "v_mfma_f32_16x16x32_bf16 v[4:7], v[136:139], v[116:119], v[4:7]\n"
    "v_mfma_f32_16x16x32_bf16 v[24:27], v[136:139], v[120:123], v[24:27]\n"
    "ds_read_b128 v[84:87], %5 offset:51200\n"
    "v_mfma_f32_16x16x32_bf16 v[44:47], v[136:139], v[124:127], v[44:47]\n"
    "v_mfma_f32_16x16x32_bf16 v[64:67], v[136:139], v[128:131], v[64:67]\n"
    "ds_read_b128 v[88:91], %5 offset:53248\n"
    "v_mfma_f32_16x16x32_bf16 v[8:11], v[140:143], v[116:119], v[8:11]\n"
    "v_mfma_f32_16x16x32_bf16 v[28:31], v[140:143], v[120:123], v[28:31]\n"
    "ds_read_b128 v[92:95], %5 offset:55296\n"
    "v_mfma_f32_16x16x32_bf16 v[48:51], v[140:143], v[124:127], v[48:51]\n"
    "v_mfma_f32_16x16x32_bf16 v[68:71], v[140:143], v[128:131], v[68:71]\n"
    "ds_read_b128 v[96:99], %8 offset:0\n"
    "v_mfma_f32_16x16x32_bf16 v[12:15], v[144:147], v[116:119], v[12:15]\n"
    "v_mfma_f32_16x16x32_bf16 v[32:35], v[144:147], v[120:123], v[32:35]\n"
    "ds_read_b128 v[100:103], %8 offset:2048\n"
    "v_mfma_f32_16x16x32_bf16 v[52:55], v[144:147], v[124:127], v[52:55]\n"
    "v_mfma_f32_16x16x32_bf16 v[72:75], v[144:147], v[128:131], v[72:75]\n"
    "ds_read_b128 v[104:107], %8 offset:4096\n"
    "v_mfma_f32_16x16x32_bf16 v[16:19], v[148:151], v[116:119], v[16:19]\n"
    "v_mfma_f32_16x16x32_bf16 v[36:39], v[148:151], v[120:123], v[36:39]\n"
    "ds_read_b128 v[108:111], %8 offset:6144\n"
    "v_mfma_f32_16x16x32_bf16 v[56:59], v[148:151], v[124:127], v[56:59]\n"
    "v_mfma_f32_16x16x32_bf16 v[76:79], v[148:151], v[128:131], v[76:79]\n"
    "ds_read_b128 v[112:115], %8 offset:8192\n"
    "s_waitcnt lgkmcnt(4)\n"
    "v_mfma_f32_16x16x32_bf16 v[0:3], v[96:99], v[80:83], v[0:3]\n"
    "ds_read_b128 v[116:119], %6 offset:49152\n"
    "v_mfma_f32_16x16x32_bf16 v[20:23], v[96:99], v[84:87], v[20:23]\n"
    "v_mfma_f32_16x16x32_bf16 v[40:43], v[96:99], v[88:91], v[40:43]\n"
    "ds_read_b128 v[120:123], %6 offset:51200\n"
    "v_mfma_f32_16x16x32_bf16 v[60:63], v[96:99], v[92:95], v[60:63]\n"
    "s_waitcnt lgkmcnt(5)\n"
    "v_mfma_f32_16x16x32_bf16 v[4:7], v[100:103], v[80:83], v[4:7]\n"
    "ds_read_b128 v[124:127], %6 offset:53248\n"
    "v_mfma_f32_16x16x32_bf16 v[24:27], v[100:103], v[84:87], v[24:27]\n"
    "v_mfma_f32_16x16x32_bf16 v[44:47], v[100:103], v[88:91], v[44:47]\n"
    "ds_read_b128 v[128:131], %6 offset:55296\n"
    "v_mfma_f32_16x16x32_bf16 v[64:67], v[100:103], v[92:95], v[64:67]\n"
    "s_waitcnt lgkmcnt(6)\n"
    "v_mfma_f32_16x16x32_bf16 v[8:11], v[104:107], v[80:83], v[8:11]\n"
    "ds_read_b128 v[132:135], %8 offset:1024\n"
    "v_mfma_f32_16x16x32_bf16 v[28:31], v[104:107], v[84:87], v[28:31]\n"
    "v_mfma_f32_16x16x32_bf16 v[48:51], v[104:107], v[88:91], v[48:51]\n"
    "ds_read_b128 v[136:139], %8 offset:3072\n"
    "v_mfma_f32_16x16x32_bf16 v[68:71], v[104:107], v[92:95], v[68:71]\n"
    "s_waitcnt lgkmcnt(7)\n"
    "v_mfma_f32_16x16x32_bf16 v[12:15], v[108:111], v[80:83], v[12:15]\n"
    "ds_read_b128 v[140:143], %8 offset:5120\n"
    "v_mfma_f32_16x16x32_bf16 v[32:35], v[108:111], v[84:87], v[32:35]\n"
    "v_mfma_f32_16x16x32_bf16 v[52:55], v[108:111], v[88:91], v[52:55]\n"
    "ds_read_b128 v[144:147], %8 offset:7168\n"
    "v_mfma_f32_16x16x32_bf16 v[72:75], v[108:111], v[92:95], v[72:75]\n"
    "s_waitcnt lgkmcnt(8)\n"
    "v_mfma_f32_16x16x32_bf16 v[16:19], v[112:115], v[80:83], v[16:19]\n"
    "ds_read_b128 v[148:151], %8 offset:9216\n"
    "v_mfma_f32_16x16x32_bf16 v[36:39], v[112:115], v[84:87], v[36:39]\n"
    "v_mfma_f32_16x16x32_bf16 v[56:59], v[112:115], v[88:91], v[56:59]\n"
    "v_mfma_f32_16x16x32_bf16 v[76:79], v[112:115], v[92:95], v[76:79]\n"
    "s_waitcnt lgkmcnt(4)\n"
    "v_mfma_f32_16x16x32_bf16 v[0:3], v[132:135], v[116:119], v[0:3]\n"
    "v_mfma_f32_16x16x32_bf16 v[20:23], v[132:135], v[120:123], v[20:23]\n"
    "s_waitcnt lgkmcnt(0)\n"
    "v_mfma_f32_16x16x32_bf16 v[40:43], v[132:135], v[124:127], v[40:43]\n"
    "s_barrier\n"
    "v_mfma_f32_16x16x32_bf16 v[60:63], v[132:135], v[128:131], v[60:63]\n"
    "ds_read_b128 v[80:83], %5 offset:16384\n"
    "v_mfma_f32_16x16x32_bf16 v[4:7], v[136:139], v[116:119], v[4:7]\n"
    "v_mfma_f32_16x16x32_bf16 v[24:27], v[136:139], v[120:123], v[24:27]\n"
    "ds_read_b128 v[84:87], %5 offset:18432\n"
    "v_mfma_f32_16x16x32_bf16 v[44:47], v[136:139], v[124:127], v[44:47]\n"
    "v_mfma_f32_16x16x32_bf16 v[64:67], v[136:139], v[128:131], v[64:67]\n"
    "ds_read_b128 v[88:91], %5 offset:20480\n"
    "v_mfma_f32_16x16x32_bf16 v[8:11], v[140:143], v[116:119], v[8:11]\n"
    "v_mfma_f32_16x16x32_bf16 v[28:31], v[140:143], v[120:123], v[28:31]\n"
    "ds_read_b128 v[92:95], %5 offset:22528\n"
    "v_mfma_f32_16x16x32_bf16 v[48:51], v[140:143], v[124:127], v[48:51]\n"
    "v_mfma_f32_16x16x32_bf16 v[68:71], v[140:143], v[128:131], v[68:71]\n"
    "ds_read_b128 v[96:99], %7 offset:0\n"
    "v_mfma_f32_16x16x32_bf16 v[12:15], v[144:147], v[116:119], v[12:15]\n"
    "v_mfma_f32_16x16x32_bf16 v[32:35], v[144:147], v[120:123], v[32:35]\n"
    "ds_read_b128 v[100:103], %7 offset:2048\n"
    "v_mfma_f32_16x16x32_bf16 v[52:55], v[144:147], v[124:127], v[52:55]\n"
    "v_mfma_f32_16x16x32_bf16 v[72:75], v[144:147], v[128:131], v[72:75]\n"
    "ds_read_b128 v[104:107], %7 offset:4096\n"
    "v_mfma_f32_16x16x32_bf16 v[16:19], v[148:151], v[116:119], v[16:19]\n"
    "v_mfma_f32_16x16x32_bf16 v[36:39], v[148:151], v[120:123], v[36:39]\n"
    "ds_read_b128 v[108:111], %7 offset:6144\n"
    "v_mfma_f32_16x16x32_bf16 v[56:59], v[148:151], v[124:127], v[56:59]\n"
    "v_mfma_f32_16x16x32_bf16 v[76:79], v[148:151], v[128:131], v[76:79]\n"
    "ds_read_b128 v[112:115], %7 offset:8192\n"
    "s_sub_u32 s40, s40, 1\n"
    "s_cmp_lg_u32 s40, 0\n"
    "s_cbranch_scc1 .Lloopc%=\n"
    "s_waitcnt lgkmcnt(4)\n"
    "v_mfma_f32_16x16x32_bf16 v[0:3], v[96:99], v[80:83], v[0:3]\n"
    "ds_read_b128 v[116:119], %6 offset:16384\n"
    "v_mfma_f32_16x16x32_bf16 v[20:23], v[96:99], v[84:87], v[20:23]\n"
    "v_mfma_f32_16x16x32_bf16 v[40:43], v[96:99], v[88:91], v[40:43]\n"
    "ds_read_b128 v[120:123], %6 offset:18432\n"
    "v_mfma_f32_16x16x32_bf16 v[60:63], v[96:99], v[92:95], v[60:63]\n"
    "s_waitcnt lgkmcnt(5)\n"
    "v_mfma_f32_16x16x32_bf16 v[4:7], v[100:103], v[80:83], v[4:7]\n"
    "ds_read_b128 v[124:127], %6 offset:20480\n"
    "v_mfma_f32_16x16x32_bf16 v[24:27], v[100:103], v[84:87], v[24:27]\n"
    "v_mfma_f32_16x16x32_bf16 v[44:47], v[100:103], v[88:91], v[44:47]\n"
    "ds_read_b128 v[128:131], %6 offset:22528\n"
    "v_mfma_f32_16x16x32_bf16 v[64:67], v[100:103], v[92:95], v[64:67]\n"
    "s_waitcnt lgkmcnt(6)\n"
    "v_mfma_f32_16x16x32_bf16 v[8:11], v[104:107], v[80:83], v[8:11]\n"
    "ds_read_b128 v[132:135], %7 offset:1024\n"
    "v_mfma_f32_16x16x32_bf16 v[28:31], v[104:107], v[84:87], v[28:31]\n"
    "v_mfma_f32_16x16x32_bf16 v[48:51], v[104:107], v[88:91], v[48:51]\n"
    "ds_read_b128 v[136:139], %7 offset:3072\n"
    "v_mfma_f32_16x16x32_bf16 v[68:71], v[104:107], v[92:95], v[68:71]\n"
    "s_waitcnt lgkmcnt(7)\n"
    "v_mfma_f32_16x16x32_bf16 v[12:15], v[108:111], v[80:83], v[12:15]\n"
    "ds_read_b128 v[140:143], %7 offset:5120\n"
    "v_mfma_f32_16x16x32_bf16 v[32:35], v[108:111], v[84:87], v[32:35]\n"
    "v_mfma_f32_16x16x32_bf16 v[52:55], v[108:111], v[88:91], v[52:55]\n"
    "ds_read_b128 v[144:147], %7 offset:7168\n"
    "v_mfma_f32_16x16x32_bf16 v[72:75], v[108:111], v[92:95], v[72:75]\n"
    "s_waitcnt lgkmcnt(8)\n"
    "v_mfma_f32_16x16x32_bf16 v[16:19], v[112:115], v[80:83], v[16:19]\n"
    "ds_read_b128 v[148:151], %7 offset:9216\n"
    "v_mfma_f32_16x16x32_bf16 v[36:39], v[112:115], v[84:87], v[36:39]\n"
    "v_mfma_f32_16x16x32_bf16 v[56:59], v[112:115], v[88:91], v[56:59]\n"
    "v_mfma_f32_16x16x32_bf16 v[76:79], v[112:115], v[92:95], v[76:79]\n"
    "s_waitcnt lgkmcnt(4)\n"
    "v_mfma_f32_16x16x32_bf16 v[0:3], v[132:135], v[116:119], v[0:3]\n"
    "v_mfma_f32_16x16x32_bf16 v[20:23], v[132:135], v[120:123], v[20:23]\n"
    "v_mfma_f32_16x16x32_bf16 v[40:43], v[132:135], v[124:127], v[40:43]\n"
    "s_waitcnt lgkmcnt(0)\n"
    "s_barrier\n"
    "v_mfma_f32_16x16x32_bf16 v[60:63], v[132:135], v[128:131], v[60:63]\n"
    "ds_read_b128 v[80:83], %5 offset:38912\n"
    "v_mfma_f32_16x16x32_bf16 v[4:7], v[136:139], v[116:119], v[4:7]\n"
    "ds_read_b128 v[96:99], %7 offset:20480\n"
    "v_mfma_f32_16x16x32_bf16 v[24:27], v[136:139], v[120:123], v[24:27]\n"
    "ds_read_b128 v[100:103], %7 offset:22528\n"
    "v_mfma_f32_16x16x32_bf16 v[44:47], v[136:139], v[124:127], v[44:47]\n"
    "ds_read_b128 v[104:107], %7 offset:24576\n"
    "v_mfma_f32_16x16x32_bf16 v[64:67], v[136:139], v[128:131], v[64:67]\n"
    "ds_read_b128 v[108:111], %7 offset:26624\n"
    "v_mfma_f32_16x16x32_bf16 v[8:11], v[140:143], v[116:119], v[8:11]\n"
    "ds_read_b128 v[112:115], %7 offset:28672\n"
    "v_mfma_f32_16x16x32_bf16 v[28:31], v[140:143], v[120:123], v[28:31]\n"
    "ds_read_b128 v[84:87], %6 offset:38912\n"
    "v_mfma_f32_16x16x32_bf16 v[48:51], v[140:143], v[124:127], v[48:51]\n"
    "ds_read_b128 v[152:155], %7 offset:21504\n"
    "v_mfma_f32_16x16x32_bf16 v[68:71], v[140:143], v[128:131], v[68:71]\n"
    "ds_read_b128 v[156:159], %7 offset:23552\n"
    "v_mfma_f32_16x16x32_bf16 v[12:15], v[144:147], v[116:119], v[12:15]\n"
    "ds_read_b128 v[160:163], %7 offset:25600\n"
    "v_mfma_f32_16x16x32_bf16 v[32:35], v[144:147], v[120:123], v[32:35]\n"
    "ds_read_b128 v[164:167], %7 offset:27648\n"
    "v_mfma_f32_16x16x32_bf16 v[52:55], v[144:147], v[124:127], v[52:55]\n"
    "ds_read_b128 v[168:171], %7 offset:29696\n"
    "v_mfma_f32_16x16x32_bf16 v[72:75], v[144:147], v[128:131], v[72:75]\n"
    "v_mfma_f32_16x16x32_bf16 v[16:19], v[148:151], v[116:119], v[16:19]\n"
    "v_mfma_f32_16x16x32_bf16 v[36:39], v[148:151], v[120:123], v[36:39]\n"
    "v_mfma_f32_16x16x32_bf16 v[56:59], v[148:151], v[124:127], v[56:59]\n"
    "v_mfma_f32_16x16x32_bf16 v[76:79], v[148:151], v[128:131], v[76:79]\n"
    "s_waitcnt lgkmcnt(10)\n"
    "v_mfma_f32_16x16x32_bf16 v[60:63], v[96:99], v[80:83], v[60:63]\n"
    "s_barrier\n"
    "ds_read_b128 v[88:91], %5 offset:6144\n"
    "s_waitcnt lgkmcnt(10)\n"
    "v_mfma_f32_16x16x32_bf16 v[64:67], v[100:103], v[80:83], v[64:67]\n"
    "ds_read_b128 v[172:175], %7 offset:40960\n"
    "s_waitcnt lgkmcnt(10)\n"
    "v_mfma_f32_16x16x32_bf16 v[68:71], v[104:107], v[80:83], v[68:71]\n"
    "ds_read_b128 v[176:179], %7 offset:43008\n"
    "s_waitcnt lgkmcnt(10)\n"
    "v_mfma_f32_16x16x32_bf16 v[72:75], v[108:111], v[80:83], v[72:75]\n"
    "ds_read_b128 v[180:183], %7 offset:45056\n"
    "s_waitcnt lgkmcnt(10)\n"
    "v_mfma_f32_16x16x32_bf16 v[76:79], v[112:115], v[80:83], v[76:79]\n"
    "ds_read_b128 v[184:187], %7 offset:47104\n"
    "s_waitcnt lgkmcnt(9)\n"
    "v_mfma_f32_16x16x32_bf16 v[60:63], v[152:155], v[84:87], v[60:63]\n"
    "ds_read_b128 v[188:191], %7 offset:49152\n"
    "s_waitcnt lgkmcnt(9)\n"
    "v_mfma_f32_16x16x32_bf16 v[64:67], v[156:159], v[84:87], v[64:67]\n"
    "ds_read_b128 v[92:95], %6 offset:6144\n"
    "s_waitcnt lgkmcnt(9)\n"
    "v_mfma_f32_16x16x32_bf16 v[68:71], v[160:163], v[84:87], v[68:71]\n"
    "ds_read_b128 v[192:195], %7 offset:41984\n"
    "s_waitcnt lgkmcnt(9)\n"
    "v_mfma_f32_16x16x32_bf16 v[72:75], v[164:167], v[84:87], v[72:75]\n"
    "ds_read_b128 v[196:199], %7 offset:44032\n"
    "s_waitcnt lgkmcnt(9)\n"
    "v_mfma_f32_16x16x32_bf16 v[76:79], v[168:171], v[84:87], v[76:79]\n"
    "ds_read_b128 v[200:203], %7 offset:46080\n"
    "s_waitcnt lgkmcnt(8)\n"
    "v_mfma_f32_16x16x32_bf16 v[60:63], v[172:175], v[88:91], v[60:63]\n"
    "ds_read_b128 v[204:207], %7 offset:48128\n"
    "s_waitcnt lgkmcnt(8)\n"
    "v_mfma_f32_16x16x32_bf16 v[64:67], v[176:179], v[88:91], v[64:67]\n"
    "ds_read_b128 v[208:211], %7 offset:50176\n"
    "s_waitcnt lgkmcnt(8)\n"
    "v_mfma_f32_16x16x32_bf16 v[68:71], v[180:183], v[88:91], v[68:71]\n"
    "s_barrier\n"
    "ds_read_b128 v[116:119], %5 offset:55296\n"
    "s_waitcnt lgkmcnt(8)\n"
    "v_mfma_f32_16x16x32_bf16 v[72:75], v[184:187], v[88:91], v[72:75]\n"
    "ds_read_b128 v[212:215], %8 offset:0\n"
    "s_waitcnt lgkmcnt(8)\n"
    "v_mfma_f32_16x16x32_bf16 v[76:79], v[188:191], v[88:91], v[76:79]\n"
    "ds_read_b128 v[216:219], %8 offset:2048\n"
    "s_waitcnt lgkmcnt(7)\n"
    "v_mfma_f32_16x16x32_bf16 v[60:63], v[192:195], v[92:95], v[60:63]\n"
    "ds_read_b128 v[220:223], %8 offset:4096\n"
    "s_waitcnt lgkmcnt(7)\n"
    "v_mfma_f32_16x16x32_bf16 v[64:67], v[196:199], v[92:95], v[64:67]\n"
    "ds_read_b128 v[224:227], %8 offset:6144\n"
    "s_waitcnt lgkmcnt(7)\n"
    "v_mfma_f32_16x16x32_bf16 v[68:71], v[200:203], v[92:95], v[68:71]\n"
    "ds_read_b128 v[228:231], %8 offset:8192\n"
    "s_waitcnt lgkmcnt(7)\n"
    "v_mfma_f32_16x16x32_bf16 v[72:75], v[204:207], v[92:95], v[72:75]\n"
    "ds_read_b128 v[120:123], %6 offset:55296\n"
    "s_waitcnt lgkmcnt(7)\n"
    "v_mfma_f32_16x16x32_bf16 v[76:79], v[208:211], v[92:95], v[76:79]\n"
    "ds_read_b128 v[132:135], %8 offset:1024\n"
    "s_waitcnt lgkmcnt(6)\n"
    "v_mfma_f32_16x16x32_bf16 v[60:63], v[212:215], v[116:119], v[60:63]\n"
    "ds_read_b128 v[136:139], %8 offset:3072\n"
    "s_waitcnt lgkmcnt(6)\n"
    "v_mfma_f32_16x16x32_bf16 v[64:67], v[216:219], v[116:119], v[64:67]\n"
    "ds_read_b128 v[140:143], %8 offset:5120\n"
    "s_waitcnt lgkmcnt(6)\n"
    "v_mfma_f32_16x16x32_bf16 v[68:71], v[220:223], v[116:119], v[68:71]\n"
    "ds_read_b128 v[144:147], %8 offset:7168\n"
    "s_waitcnt lgkmcnt(6)\n"
    "v_mfma_f32_16x16x32_bf16 v[72:75], v[224:227], v[116:119], v[72:75]\n"
    "ds_read_b128 v[148:151], %8 offset:9216\n"
    "s_waitcnt lgkmcnt(6)\n"
    "v_mfma_f32_16x16x32_bf16 v[76:79], v[228:231], v[116:119], v[76:79]\n"
    "ds_read_b128 v[124:127], %5 offset:36864\n"
    "s_waitcnt lgkmcnt(5)\n"
    "v_mfma_f32_16x16x32_bf16 v[60:63], v[132:135], v[120:123], v[60:63]\n"
    "ds_read_b128 v[128:131], %6 offset:36864\n"
    "s_waitcnt lgkmcnt(5)\n"
    "v_mfma_f32_16x16x32_bf16 v[64:67], v[136:139], v[120:123], v[64:67]\n"
    "ds_read_b128 v[80:83], %5 offset:4096\n"
    "s_waitcnt lgkmcnt(5)\n"
    "v_mfma_f32_16x16x32_bf16 v[68:71], v[140:143], v[120:123], v[68:71]\n"
    "ds_read_b128 v[84:87], %6 offset:4096\n"
    "s_waitcnt lgkmcnt(5)\n"
    "v_mfma_f32_16x16x32_bf16 v[72:75], v[144:147], v[120:123], v[72:75]\n"
    "ds_read_b128 v[88:91], %5 offset:53248\n"
    "s_waitcnt lgkmcnt(5)\n"
    "v_mfma_f32_16x16x32_bf16 v[76:79], v[148:151], v[120:123], v[76:79]\n"
    "ds_read_b128 v[92:95], %6 offset:53248\n"
    "s_waitcnt lgkmcnt(5)\n"
    "v_mfma_f32_16x16x32_bf16 v[40:43], v[96:99], v[124:127], v[40:43]\n"
    "ds_read_b128 v[116:119], %5 offset:34816\n"
    "v_mfma_f32_16x16x32_bf16 v[44:47], v[100:103], v[124:127], v[44:47]\n"
    "v_mfma_f32_16x16x32_bf16 v[48:51], v[104:107], v[124:127], v[48:51]\n"
    "ds_read_b128 v[120:123], %6 offset:34816\n"
    "v_mfma_f32_16x16x32_bf16 v[52:55], v[108:111], v[124:127], v[52:55]\n"
    "v_mfma_f32_16x16x32_bf16 v[56:59], v[112:115], v[124:127], v[56:59]\n"
    "v_cvt_pk_bf16_f32 v60, v60, v61\n"
    "v_cvt_pk_bf16_f32 v61, v62, v63\n"
    "s_waitcnt lgkmcnt(6)\n"
    "v_mfma_f32_16x16x32_bf16 v[40:43], v[152:155], v[128:131], v[40:43]\n"
    "v_cvt_pk_bf16_f32 v62, v64, v65\n"
    "v_cvt_pk_bf16_f32 v63, v66, v67\n"
    "v_mfma_f32_16x16x32_bf16 v[44:47], v[156:159], v[128:131], v[44:47]\n"
    "ds_read_b128 v[124:127], %5 offset:2048\n"
    "buffer_store_dwordx4 v[60:63], %9, %15, %21 offen sc1\n"
    "v_mfma_f32_16x16x32_bf16 v[48:51], v[160:163], v[128:131], v[48:51]\n"
    "v_cvt_pk_bf16_f32 v64, v68, v69\n"
    "v_cvt_pk_bf16_f32 v65, v70, v71\n"
    "v_mfma_f32_16x16x32_bf16 v[52:55], v[164:167], v[128:131], v[52:55]\n"
    "v_cvt_pk_bf16_f32 v66, v72, v73\n"
    "v_cvt_pk_bf16_f32 v67, v74, v75\n"
    "v_mfma_f32_16x16x32_bf16 v[56:59], v[168:171], v[128:131], v[56:59]\n"
    "buffer_store_dwordx4 v[64:67], %9, %15, %21 offen offset:1024 sc1\n"
    "v_cvt_pk_bf16_f32 v68, v76, v77\n"
    "s_waitcnt lgkmcnt(6)\n"
    "v_mfma_f32_16x16x32_bf16 v[40:43], v[172:175], v[80:83], v[40:43]\n"
    "ds_read_b128 v[128:131], %6 offset:2048\n"
    "v_cvt_pk_bf16_f32 v69, v78, v79\n"
    "v_mfma_f32_16x16x32_bf16 v[44:47], v[176:179], v[80:83], v[44:47]\n"
    "buffer_store_dwordx2 v[68:69], %10, %15, %21 offen offset:2048 sc1\n"
    "v_mfma_f32_16x16x32_bf16 v[48:51], v[180:183], v[80:83], v[48:51]\n"
    "v_mfma_f32_16x16x32_bf16 v[52:55], v[184:187], v[80:83], v[52:55]\n"
    "v_mfma_f32_16x16x32_bf16 v[56:59], v[188:191], v[80:83], v[56:59]\n"
    "s_waitcnt lgkmcnt(6)\n"
    "v_mfma_f32_16x16x32_bf16 v[40:43], v[192:195], v[84:87], v[40:43]\n"
    "v_mfma_f32_16x16x32_bf16 v[44:47], v[196:199], v[84:87], v[44:47]\n"
    "ds_read_b128 v[80:83], %5 offset:51200\n"
    "v_mfma_f32_16x16x32_bf16 v[48:51], v[200:203], v[84:87], v[48:51]\n"
    "v_mfma_f32_16x16x32_bf16 v[52:55], v[204:207], v[84:87], v[52:55]\n"
    "v_mfma_f32_16x16x32_bf16 v[56:59], v[208:211], v[84:87], v[56:59]\n"
    "s_waitcnt lgkmcnt(6)\n"
    "v_mfma_f32_16x16x32_bf16 v[40:43], v[212:215], v[88:91], v[40:43]\n"
    "ds_read_b128 v[84:87], %6 offset:51200\n"
    "v_mfma_f32_16x16x32_bf16 v[44:47], v[216:219], v[88:91], v[44:47]\n"
    "v_mfma_f32_16x16x32_bf16 v[48:51], v[220:223], v[88:91], v[48:51]\n"
    "v_mfma_f32_16x16x32_bf16 v[52:55], v[224:227], v[88:91], v[52:55]\n"
    "v_mfma_f32_16x16x32_bf16 v[56:59], v[228:231], v[88:91], v[56:59]\n"
    "s_waitcnt lgkmcnt(6)\n"
    "v_mfma_f32_16x16x32_bf16 v[40:43], v[132:135], v[92:95], v[40:43]\n"
    "v_mfma_f32_16x16x32_bf16 v[44:47], v[136:139], v[92:95], v[44:47]\n"
    "ds_read_b128 v[88:91], %5 offset:32768\n"
    "v_mfma_f32_16x16x32_bf16 v[48:51], v[140:143], v[92:95], v[48:51]\n"
    "v_mfma_f32_16x16x32_bf16 v[52:55], v[144:147], v[92:95], v[52:55]\n"
    "v_mfma_f32_16x16x32_bf16 v[56:59], v[148:151], v[92:95], v[56:59]\n"
    "s_waitcnt lgkmcnt(6)\n"
    "v_mfma_f32_16x16x32_bf16 v[20:23], v[96:99], v[116:119], v[20:23]\n"
    "ds_read_b128 v[92:95], %6 offset:32768\n"
    "v_mfma_f32_16x16x32_bf16 v[24:27], v[100:103], v[116:119], v[24:27]\n"
    "v_mfma_f32_16x16x32_bf16 v[28:31], v[104:107], v[116:119], v[28:31]\n"
    "v_mfma_f32_16x16x32_bf16 v[32:35], v[108:111], v[116:119], v[32:35]\n"
    "v_mfma_f32_16x16x32_bf16 v[36:39], v[112:115], v[116:119], v[36:39]\n"
    "v_cvt_pk_bf16_f32 v40, v40, v41\n"
    "v_cvt_pk_bf16_f32 v41, v42, v43\n"
    "s_waitcnt lgkmcnt(6)\n"
    "v_mfma_f32_16x16x32_bf16 v[20:23], v[152:155], v[120:123], v[20:23]\n"
    "v_cvt_pk_bf16_f32 v42, v44, v45\n"
    "v_cvt_pk_bf16_f32 v43, v46, v47\n"
    "v_mfma_f32_16x16x32_bf16 v[24:27], v[156:159], v[120:123], v[24:27]\n"
    "ds_read_b128 v[116:119], %5 offset:0\n"
    "buffer_store_dwordx4 v[40:43], %9, %15, %20 offen sc1\n"
    "v_mfma_f32_16x16x32_bf16 v[28:31], v[160:163], v[120:123], v[28:31]\n"
    "v_cvt_pk_bf16_f32 v44, v48, v49\n"
    "v_cvt_pk_bf16_f32 v45, v50, v51\n"
    "v_mfma_f32_16x16x32_bf16 v[32:35], v[164:167], v[120:123], v[32:35]\n"
    "v_cvt_pk_bf16_f32 v46, v52, v53\n"
    "v_cvt_pk_bf16_f32 v47, v54, v55\n"
    "v_mfma_f32_16x16x32_bf16 v[36:39], v[168:171], v[120:123], v[36:39]\n"
    "buffer_store_dwordx4 v[44:47], %9, %15, %20 offen offset:1024 sc1\n"
    "v_cvt_pk_bf16_f32 v48, v56, v57\n"
    "s_waitcnt lgkmcnt(6)\n"
    "v_mfma_f32_16x16x32_bf16 v[20:23], v[172:175], v[124:127], v[20:23]\n"
    "ds_read_b128 v[120:123], %6 offset:0\n"
    "v_cvt_pk_bf16_f32 v49, v58, v59\n"
    "v_mfma_f32_16x16x32_bf16 v[24:27], v[176:179], v[124:127], v[24:27]\n"
    "buffer_store_dwordx2 v[48:49], %10, %15, %20 offen offset:2048 sc1\n"
    "v_mfma_f32_16x16x32_bf16 v[28:31], v[180:183], v[124:127], v[28:31]\n"
    "v_mfma_f32_16x16x32_bf16 v[32:35], v[184:187], v[124:127], v[32:35]\n"
    "v_mfma_f32_16x16x32_bf16 v[36:39], v[188:191], v[124:127], v[36:39]\n"
    "s_waitcnt lgkmcnt(6)\n"
    "v_mfma_f32_16x16x32_bf16 v[20:23], v[192:195], v[128:131], v[20:23]\n"
    "v_mfma_f32_16x16x32_bf16 v[24:27], v[196:199], v[128:131], v[24:27]\n"
    "ds_read_b128 v[124:127], %5 offset:49152\n"
    "v_mfma_f32_16x16x32_bf16 v[28:31], v[200:203], v[128:131], v[28:31]\n"
    "v_mfma_f32_16x16x32_bf16 v[32:35], v[204:207], v[128:131], v[32:35]\n"
    "v_mfma_f32_16x16x32_bf16 v[36:39], v[208:211], v[128:131], v[36:39]\n"
    "s_waitcnt lgkmcnt(6)\n"
    "v_mfma_f32_16x16x32_bf16 v[20:23], v[212:215], v[80:83], v[20:23]\n"
    "ds_read_b128 v[128:131], %6 offset:49152\n"
    "v_mfma_f32_16x16x32_bf16 v[24:27], v[216:219], v[80:83], v[24:27]\n"
    "v_mfma_f32_16x16x32_bf16 v[28:31], v[220:223], v[80:83], v[28:31]\n"
    "v_mfma_f32_16x16x32_bf16 v[32:35], v[224:227], v[80:83], v[32:35]\n"
    "v_mfma_f32_16x16x32_bf16 v[36:39], v[228:231], v[80:83], v[36:39]\n"
    "s_waitcnt lgkmcnt(6)\n"
    "v_mfma_f32_16x16x32_bf16 v[20:23], v[132:135], v[84:87], v[20:23]\n"
    "v_mfma_f32_16x16x32_bf16 v[24:27], v[136:139], v[84:87], v[24:27]\n"
    "v_mfma_f32_16x16x32_bf16 v[28:31], v[140:143], v[84:87], v[28:31]\n"
    "v_mfma_f32_16x16x32_bf16 v[32:35], v[144:147], v[84:87], v[32:35]\n"
    "v_mfma_f32_16x16x32_bf16 v[36:39], v[148:151], v[84:87], v[36:39]\n"
    "s_waitcnt lgkmcnt(5)\n"
    "v_mfma_f32_16x16x32_bf16 v[0:3], v[96:99], v[88:91], v[0:3]\n"
    "v_mfma_f32_16x16x32_bf16 v[4:7], v[100:103], v[88:91], v[4:7]\n"
    "v_mfma_f32_16x16x32_bf16 v[8:11], v[104:107], v[88:91], v[8:11]\n"
    "v_mfma_f32_16x16x32_bf16 v[12:15], v[108:111], v[88:91], v[12:15]\n"
    "v_mfma_f32_16x16x32_bf16 v[16:19], v[112:115], v[88:91], v[16:19]\n"
    "v_cvt_pk_bf16_f32 v20, v20, v21\n"
    "v_cvt_pk_bf16_f32 v21, v22, v23\n"
    "s_waitcnt lgkmcnt(4)\n"
    "v_mfma_f32_16x16x32_bf16 v[0:3], v[152:155], v[92:95], v[0:3]\n"
    "v_cvt_pk_bf16_f32 v22, v24, v25\n"
    "v_cvt_pk_bf16_f32 v23, v26, v27\n"
    "v_mfma_f32_16x16x32_bf16 v[4:7], v[156:159], v[92:95], v[4:7]\n"
    "buffer_store_dwordx4 v[20:23], %9, %15, %19 offen sc1\n"
    "v_cvt_pk_bf16_f32 v24, v28, v29\n"
    "v_mfma_f32_16x16x32_bf16 v[8:11], v[160:163], v[92:95], v[8:11]\n"
    "v_cvt_pk_bf16_f32 v25, v30, v31\n"
    "v_cvt_pk_bf16_f32 v26, v32, v33\n"
    "v_mfma_f32_16x16x32_bf16 v[12:15], v[164:167], v[92:95], v[12:15]\n"
    "v_cvt_pk_bf16_f32 v27, v34, v35\n"
    "v_mfma_f32_16x16x32_bf16 v[16:19], v[168:171], v[92:95], v[16:19]\n"
    "buffer_store_dwordx4 v[24:27], %9, %15, %19 offen offset:1024 sc1\n"
    "v_cvt_pk_bf16_f32 v28, v36, v37\n"
    "s_waitcnt lgkmcnt(3)\n"
    "v_mfma_f32_16x16x32_bf16 v[0:3], v[172:175], v[116:119], v[0:3]\n"
    "v_cvt_pk_bf16_f32 v29, v38, v39\n"
    "v_mfma_f32_16x16x32_bf16 v[4:7], v[176:179], v[116:119], v[4:7]\n"
    "buffer_store_dwordx2 v[28:29], %10, %15, %19 offen offset:2048 sc1\n"
    "v_mfma_f32_16x16x32_bf16 v[8:11], v[180:183], v[116:119], v[8:11]\n"
    "v_mfma_f32_16x16x32_bf16 v[12:15], v[184:187], v[116:119], v[12:15]\n"
    "v_mfma_f32_16x16x32_bf16 v[16:19], v[188:191], v[116:119], v[16:19]\n"
    "s_waitcnt lgkmcnt(2)\n"
    "v_mfma_f32_16x16x32_bf16 v[0:3], v[192:195], v[120:123], v[0:3]\n"
    "v_mfma_f32_16x16x32_bf16 v[4:7], v[196:199], v[120:123], v[4:7]\n"
    "v_mfma_f32_16x16x32_bf16 v[8:11], v[200:203], v[120:123], v[8:11]\n"
    "v_mfma_f32_16x16x32_bf16 v[12:15], v[204:207], v[120:123], v[12:15]\n"
    "v_mfma_f32_16x16x32_bf16 v[16:19], v[208:211], v[120:123], v[16:19]\n"
    "s_waitcnt lgkmcnt(1)\n"
    "v_mfma_f32_16x16x32_bf16 v[0:3], v[212:215], v[124:127], v[0:3]\n"
    "v_mfma_f32_16x16x32_bf16 v[4:7], v[216:219], v[124:127], v[4:7]\n"
    "v_mfma_f32_16x16x32_bf16 v[8:11], v[220:223], v[124:127], v[8:11]\n"
    "v_mfma_f32_16x16x32_bf16 v[12:15], v[224:227], v[124:127], v[12:15]\n"
    "v_mfma_f32_16x16x32_bf16 v[16:19], v[228:231], v[124:127], v[16:19]\n"
    "s_waitcnt lgkmcnt(0)\n"
    "v_mfma_f32_16x16x32_bf16 v[0:3], v[132:135], v[128:131], v[0:3]\n"
    "v_mfma_f32_16x16x32_bf16 v[4:7], v[136:139], v[128:131], v[4:7]\n"
    "v_mfma_f32_16x16x32_bf16 v[8:11], v[140:143], v[128:131], v[8:11]\n"
    "v_mfma_f32_16x16x32_bf16 v[12:15], v[144:147], v[128:131], v[12:15]\n"
    "v_mfma_f32_16x16x32_bf16 v[16:19], v[148:151], v[128:131], v[16:19]\n"
    "s_waitcnt vmcnt(3)\n"
    "s_mov_b64 exec, 6\n"
    "buffer_atomic_add %12, %11, %15, 0 offen sc1\n"
    "s_mov_b32 s42, 0\n"
    "s_mov_b32 s43, 0x10000\n"
    "s_mov_b32 s49, 0\n"
    "s_add_u32 s50, %24, 10240\n"
    "s_add_u32 s51, %24, 20480\n"
    "s_waitcnt vmcnt(1)\n"
    "s_mov_b64 exec, 1\n"
    "buffer_atomic_add %12, %11, %15, 0 offen sc1\n"
    "s_mov_b64 exec, -1\n"
    ".Lx%=:\n"
    "s_mov_b32 s45, s42\n"
    "s_cmp_eq_u32 s42, 7\n"
    "s_cbranch_scc1 .Lw%=\n"
    "buffer_load_dword v80, off, %15, %23 sc1\n"
    "s_sub_u32 s43, s43, 1\n"
    "s_cselect_b32 s52, 7, 0\n"
    "s_waitcnt vmcnt(0)\n"
    "v_readfirstlane_b32 s44, v80\n"
    "s_or_b32 s44, s44, s52\n"
    "s_or_b32 s42, s42, s44\n"
    "s_andn2_b32 s44, s42, s45\n"
    "s_bitcmp1_b32 s44, 0\n"
    "s_cbranch_scc0 .Ll0%=\n"
    "buffer_load_dwordx4 v[84:87], %9, %15, %24 offen sc1\n"
    "buffer_load_dwordx4 v[88:91], %9, %15, %24 offen offset:1024 sc1\n"
    "buffer_load_dwordx2 v[92:93], %10, %15, %24 offen offset:2048 sc1\n"
    ".Ll0%=:\n"
    "s_bitcmp1_b32 s44, 1\n"
    "s_cbranch_scc0 .Ll1%=\n"
    "buffer_load_dwordx4 v[96:99], %9, %15, s50 offen sc1\n"
    "buffer_load_dwordx4 v[100:103], %9, %15, s50 offen offset:1024 sc1\n"
    "buffer_load_dwordx2 v[104:105], %10, %15, s50 offen offset:2048 sc1\n"
    ".Ll1%=:\n"
    "s_bitcmp1_b32 s44, 2\n"
    "s_cbranch_scc0 .Ll2%=\n"
    "buffer_load_dwordx4 v[108:111], %9, %15, s51 offen sc1\n"
    "buffer_load_dwordx4 v[112:115], %9, %15, s51 offen offset:1024 sc1\n"
    "buffer_load_dwordx2 v[116:117], %10, %15, s51 offen offset:2048 sc1\n"
    ".Ll2%=:\n"
    "s_branch .La%=\n"
    ".Lw%=:\n"
    "s_waitcnt vmcnt(0)\n"
    ".La%=:\n"
    "s_cmp_lg_u32 s49, 0\n"
    "s_cbranch_scc1 .Lb2%=\n"
    "s_bitcmp1_b32 s45, 2\n"
    "s_cbranch_scc0 .Lx%=\n"
    "v_lshlrev_b32 v120, 16, v108\n"
    "v_and_b32 v121, 0xffff0000, v108\n"
    "v_pk_add_f32 v[0:1], v[0:1], v[120:121]\n"
    "v_lshlrev_b32 v122, 16, v109\n"
    "v_and_b32 v123, 0xffff0000, v109\n"
    "v_pk_add_f32 v[2:3], v[2:3], v[122:123]\n"
    "v_lshlrev_b32 v124, 16, v110\n"
    "v_and_b32 v125, 0xffff0000, v110\n"
    "v_pk_add_f32 v[4:5], v[4:5], v[124:125]\n"
    "v_lshlrev_b32 v126, 16, v111\n"
    "v_and_b32 v127, 0xffff0000, v111\n"
    "v_pk_add_f32 v[6:7], v[6:7], v[126:127]\n"
    "v_lshlrev_b32 v120, 16, v112\n"
    "v_and_b32 v121, 0xffff0000, v112\n"
    "v_pk_add_f32 v[8:9], v[8:9], v[120:121]\n"
    "v_lshlrev_b32 v122, 16, v113\n"
    "v_and_b32 v123, 0xffff0000, v113\n"
    "v_pk_add_f32 v[10:11], v[10:11], v[122:123]\n"
    "v_lshlrev_b32 v124, 16, v114\n"
    "v_and_b32 v125, 0xffff0000, v114\n"
    "v_pk_add_f32 v[12:13], v[12:13], v[124:125]\n"
    "v_lshlrev_b32 v126, 16, v115\n"
    "v_and_b32 v127, 0xffff0000, v115\n"
    "v_pk_add_f32 v[14:15], v[14:15], v[126:127]\n"
    "v_lshlrev_b32 v120, 16, v116\n"
    "v_and_b32 v121, 0xffff0000, v116\n"
    "v_pk_add_f32 v[16:17], v[16:17], v[120:121]\n"
    "v_lshlrev_b32 v122, 16, v117\n"
    "v_and_b32 v123, 0xffff0000, v117\n"
    "v_pk_add_f32 v[18:19], v[18:19], v[122:123]\n"
    "s_mov_b32 s49, 1\n"
    ".Lb2%=:\n"
    "s_cmp_lg_u32 s49, 1\n"
    "s_cbranch_scc1 .Lb1%=\n"
    "s_bitcmp1_b32 s45, 1\n"
    "s_cbranch_scc0 .Lx%=\n"
    "v_lshlrev_b32 v120, 16, v96\n"
    "v_and_b32 v121, 0xffff0000, v96\n"
    "v_pk_add_f32 v[0:1], v[0:1], v[120:121]\n"
    "v_lshlrev_b32 v122, 16, v97\n"
    "v_and_b32 v123, 0xffff0000, v97\n"
    "v_pk_add_f32 v[2:3], v[2:3], v[122:123]\n"
    "v_lshlrev_b32 v124, 16, v98\n"
    "v_and_b32 v125, 0xffff0000, v98\n"
    "v_pk_add_f32 v[4:5], v[4:5], v[124:125]\n"
    "v_lshlrev_b32 v126, 16, v99\n"
    "v_and_b32 v127, 0xffff0000, v99\n"
    "v_pk_add_f32 v[6:7], v[6:7], v[126:127]\n"
    "v_lshlrev_b32 v120, 16, v100\n"
    "v_and_b32 v121, 0xffff0000, v100\n"
    "v_pk_add_f32 v[8:9], v[8:9], v[120:121]\n"
    "v_lshlrev_b32 v122, 16, v101\n"
    "v_and_b32 v123, 0xffff0000, v101\n"
    "v_pk_add_f32 v[10:11], v[10:11], v[122:123]\n"
    "v_lshlrev_b32 v124, 16, v102\n"
    "v_and_b32 v125, 0xffff0000, v102\n"
    "v_pk_add_f32 v[12:13], v[12:13], v[124:125]\n"
    "v_lshlrev_b32 v126, 16, v103\n"
    "v_and_b32 v127, 0xffff0000, v103\n"
    "v_pk_add_f32 v[14:15], v[14:15], v[126:127]\n"
    "v_lshlrev_b32 v120, 16, v104\n"
    "v_and_b32 v121, 0xffff0000, v104\n"
    "v_pk_add_f32 v[16:17], v[16:17], v[120:121]\n"
    "v_lshlrev_b32 v122, 16, v105\n"
    "v_and_b32 v123, 0xffff0000, v105\n"
    "v_pk_add_f32 v[18:19], v[18:19], v[122:123]\n"
    "s_mov_b32 s49, 2\n"
    ".Lb1%=:\n"
    "s_cmp_lg_u32 s49, 2\n"
    "s_cbranch_scc1 .Lb0%=\n"
    "s_bitcmp1_b32 s45, 0\n"
    "s_cbranch_scc0 .Lx%=\n"
    "v_lshlrev_b32 v120, 16, v84\n"
    "v_and_b32 v121, 0xffff0000, v84\n"
    "v_pk_add_f32 v[0:1], v[0:1], v[120:121]\n"
    "v_lshlrev_b32 v122, 16, v85\n"
    "v_and_b32 v123, 0xffff0000, v85\n"
    "v_pk_add_f32 v[2:3], v[2:3], v[122:123]\n"
    "v_lshlrev_b32 v124, 16, v86\n"
    "v_and_b32 v125, 0xffff0000, v86\n"
    "v_pk_add_f32 v[4:5], v[4:5], v[124:125]\n"
    "v_lshlrev_b32 v126, 16, v87\n"
    "v_and_b32 v127, 0xffff0000, v87\n"
    "v_pk_add_f32 v[6:7], v[6:7], v[126:127]\n"
    "v_lshlrev_b32 v120, 16, v88\n"
    "v_and_b32 v121, 0xffff0000, v88\n"
    "v_pk_add_f32 v[8:9], v[8:9], v[120:121]\n"
    "v_lshlrev_b32 v122, 16, v89\n"
    "v_and_b32 v123, 0xffff0000, v89\n"
    "v_pk_add_f32 v[10:11], v[10:11], v[122:123]\n"
    "v_lshlrev_b32 v124, 16, v90\n"
    "v_and_b32 v125, 0xffff0000, v90\n"
    "v_pk_add_f32 v[12:13], v[12:13], v[124:125]\n"
    "v_lshlrev_b32 v126, 16, v91\n"
    "v_and_b32 v127, 0xffff0000, v91\n"
    "v_pk_add_f32 v[14:15], v[14:15], v[126:127]\n"
    "v_lshlrev_b32 v120, 16, v92\n"
    "v_and_b32 v121, 0xffff0000, v92\n"
    "v_pk_add_f32 v[16:17], v[16:17], v[120:121]\n"
    "v_lshlrev_b32 v122, 16, v93\n"
    "v_and_b32 v123, 0xffff0000, v93\n"
    "v_pk_add_f32 v[18:19], v[18:19], v[122:123]\n"
    "s_mov_b32 s49, 3\n"
    ".Lb0%=:\n"
    "v_cvt_pk_bf16_f32 v0, v0, v1\n"
    "v_cvt_pk_bf16_f32 v1, v2, v3\n"
    "v_cvt_pk_bf16_f32 v2, v4, v5\n"
    "v_cvt_pk_bf16_f32 v3, v6, v7\n"
    "v_cvt_pk_bf16_f32 v4, v8, v9\n"
    "v_cvt_pk_bf16_f32 v5, v10, v11\n"
    "v_cvt_pk_bf16_f32 v6, v12, v13\n"
    "v_cvt_pk_bf16_f32 v7, v14, v15\n"
    "v_cvt_pk_bf16_f32 v8, v16, v17\n"
    "v_cvt_pk_bf16_f32 v9, v18, v19\n"
    "v_permlane16_swap_b32 v0, v2\n"
    "v_permlane16_swap_b32 v1, v3\n"
    "buffer_store_dwordx4 v[0:3], %13, %16, 0 offen sc1\n"
    "v_permlane16_swap_b32 v4, v6\n"
    "v_permlane16_swap_b32 v5, v7\n"
    "buffer_store_dwordx4 v[4:7], %13, %16, 0 offen offset:64 sc1\n"
    "buffer_store_dwordx2 v[8:9], %14, %16, 0 offen offset:128 sc1\n"
      ::"v"(xa0), "v"(xa0 ^ 64), "v"(wb0), "v"(wb0 ^ 64), "v"(dvo), "v"(a0), "v"(a0 ^ 64), "v"(b0), "v"(b0 + 3 * 20480), "v"(lane * 16), "v"(lane * 8),
        "v"(myw + (((lane + 1 + q) & 3) - q) * (64 * FL_STRIDE)), "v"(1u << (lane & 31)), "v"(yo + (g & 1) * 32 + (g >> 1) * 16), "v"(yo + g * 8),
        "s"(sW), "s"(sY), "s"(part ? 0x80008000u : 0u), "s"(seq1), "s"(xo[1]), "s"(xo[2]), "s"(xo[3]), "s"(wn), "s"(myw), "s"(xo[0])
      : "memory", "scc",
"vcc", "s40", "s41", "s42", "s43", "s44", "s45", "s46", "s47", "s48", "s49", "s50", "s51", "s52", "v0", "v1", "v2", "v3", "v4", "v5", "v6", "v7", "v8", "v9", "v10", "v11", "v12", "v13", "v14", "v15", "v16", "v17", "v18", "v19", "v20", "v21", "v22", "v23", "v24", "v25", "v26", "v27", "v28", "v29", "v30", "v31", "v32", "v33", "v34", "v35", "v36", "v37", "v38", "v39", "v40", "v41", "v42", "v43", "v44", "v45", "v46", "v47", "v48", "v49", "v50", "v51", "v52", "v53", "v54", "v55", "v56", "v57", "v58", "v59", "v60", "v61", "v62", "v63", "v64", "v65", "v66", "v67", "v68", "v69", "v70", "v71", "v72", "v73", "v74", "v75", "v76", "v77", "v78", "v79", "v80", "v81", "v82", "v83", "v84", "v85", "v86", "v87", "v88", "v89", "v90", "v91", "v92", "v93", "v94", "v95", "v96", "v97", "v98", "v99", "v100", "v101", "v102", "v103", "v104", "v105", "v106", "v107", "v108", "v109", "v110", "v111", "v112", "v113", "v114", "v115", "v116", "v117", "v118", "v119", "v120", "v121", "v122", "v123", "v124", "v125", "v126", "v127", "v128", "v129", "v130", "v131", "v132", "v133", "v134", "v135", "v136", "v137", "v138", "v139", "v140", "v141", "v142", "v143", "v144", "v145", "v146", "v147", "v148", "v149", "v150", "v151", "v152", "v153", "v154", "v155", "v156", "v157", "v158", "v159", "v160", "v161", "v162", "v163", "v164", "v165", "v166", "v167", "v168", "v169", "v170", "v171", "v172", "v173", "v174", "v175", "v176", "v177", "v178", "v179", "v180", "v181", "v182", "v183", "v184", "v185", "v186", "v187", "v188", "v189", "v190", "v191", "v192", "v193", "v194", "v195", "v196", "v197", "v198", "v199", "v200", "v201", "v202", "v203", "v204", "v205", "v206", "v207", "v208", "v209", "v210", "v211", "v212", "v213", "v214", "v215", "v216", "v217", "v218", "v219", "v220", "v221", "v222", "v223", "v224", "v225", "v226", "v227", "v228", "v229", "v230", "v231");
  return;
  }

  asm volatile(
    "s_setprio 3\n"
    "s_load_dwordx2 s[40:41], %0, 0x0\n"
    "s_and_b32 s63, %1, 4\n"
    "s_lshl_b32 s63, s63, 1\n"
    "s_add_u32 s72, s63, 24\n"
    "s_load_dwordx2 s[44:45], %0, s72\n"
    "s_load_dwordx2 s[52:53], %0, 0x28\n"
    "s_add_u32 s73, s63, 8\n"
    "s_load_dwordx2 s[48:49], %0, s73\n"
    "s_mov_b32 s36, %1\n"
    "s_sub_u32 s37, %2, 4\n"
    "v_and_b32 v1, 63, %3\n"
    "v_lshrrev_b32 v2, 3, v1\n"
    "v_lshrrev_b32 v3, 4, v1\n"
    "v_and_b32 v4, 7, v1\n"
    "v_xor_b32 v3, v3, v4\n"
    "v_lshlrev_b32 v3, 4, v3\n"
    "s_and_b32 s60, s36, 2\n"
    "s_lshl_b32 s60, s60, 9\n"
    "s_and_b32 s61, s36, 1\n"
    "s_lshl_b32 s61, s61, 8\n"
    "s_or_b32 s60, s60, s61\n"
    "s_and_b32 s61, s36, 0xc0\n"
    "s_or_b32 s60, s60, s61\n"
    "s_lshl_b32 s61, s37, 4\n"
    "s_or_b32 s60, s60, s61\n"
    "v_add_u32 v4, s60, v2\n"
    "s_movk_i32 s62, 1280\n"
    "v_mad_u32_u24 v10, v4, s62, v3\n"
    "s_movk_i32 s63, 9216\n"
    "v_xad_u32 v11, v10, 64, s63\n"
    "s_bfe_u32 s87, s36, 0x30003\n"
    "s_mul_i32 s60, s87, 80\n"
    "s_and_b32 s61, s37, 1\n"
    "s_mul_i32 s64, s61, 40\n"
    "s_add_u32 s60, s60, s64\n"
    "v_add_u32 v4, s60, v2\n"
    "s_lshl_b32 s61, s61, 6\n"
    "s_and_b32 s64, s37, 2\n"
    "s_lshl_b32 s64, s64, 6\n"
    "s_or_b32 s61, s61, s64\n"
    "v_mad_u32_u24 v12, v4, s62, v3\n"
    "v_xor_b32 v12, s61, v12\n"
    "v_xad_u32 v13, v12, 64, s63\n"
    "v_add_u32 v14, 18432, v12\n"
    "v_add_u32 v15, 18432, v13\n"
    "v_add_u32 v16, 40960, v12\n"
    "s_lshl_b32 s38, s37, 11\n"
    "s_mul_i32 s39, s37, 5120\n"
    "s_add_u32 s68, s39, 65536\n"
    "s_add_u32 s39, s39, 49152\n"
    "s_mov_b32 s42, 2621440\n"
    "s_mov_b32 s43, 0x20000\n"
    "s_mov_b32 s46, 819200\n"
    "s_mov_b32 s47, 0x20000\n"
    "s_mov_b32 s54, 13148160\n"
    "s_mov_b32 s55, 0x20000\n"
    "s_cmp_eq_u32 s37, 3\n"
    "s_cselect_b64 vcc, -1, 0\n"
    "s_mov_b32 s60, 0\n"
    "s_movk_i32 s61, 128\n"
    "s_mov_b32 s62, s39\n"
    "s_mov_b32 m0, s38\n"
    "s_waitcnt lgkmcnt(0)\n"
    "s_cmp_lg_u32 s36, 192\n"
    "s_cbranch_scc1 .Lc1%=\n"
    "s_lshl_b32 s63, s37, 12\n"
    "s_add_u32 s63, s63, 13107200\n"
    "v_lshlrev_b32 v5, 4, v1\n"
    "v_mov_b64 v[44:45], 0\n"
    "v_mov_b64 v[46:47], 0\n"
    "buffer_store_dwordx4 v[44:47], v5, s[52:55], s63 offen offset:0 sc1\n"
    "buffer_store_dwordx4 v[44:47], v5, s[52:55], s63 offen offset:1024 sc1\n"
    "buffer_store_dwordx4 v[44:47], v5, s[52:55], s63 offen offset:2048 sc1\n"
    "buffer_store_dwordx4 v[44:47], v5, s[52:55], s63 offen offset:3072 sc1\n"
    ".Lc1%=:\n"
    "s_cbranch_vccz .Lc2%=\n"
    "s_lshl_b32 s84, s36, 2\n"
    "s_add_u32 s85, s84, 13144064\n"
    "buffer_load_dword v30, off, s[52:55], s85 sc1\n"
    ".Lc2%=:\n"
    "buffer_load_dwordx4 v10, s[40:43], s60 offen offset:0 lds\n"
    "buffer_load_dwordx4 v11, s[40:43], s60 offen offset:1024 lds\n"
    "s_add_u32 m0, s38, 8192\n"
    "s_nop 0\n"
    "buffer_load_dwordx4 v10, s[40:43], s61 offen offset:0 lds\n"
    "buffer_load_dwordx4 v11, s[40:43], s61 offen offset:1024 lds\n"
    "s_mov_b32 m0, s62\n"
    "s_nop 0\n"
    "buffer_load_dwordx4 v12, s[44:47], s60 offen offset:0 lds\n"
    "buffer_load_dwordx4 v13, s[44:47], s60 offen offset:1024 lds\n"
    "buffer_load_dwordx4 v14, s[44:47], s60 offen offset:2048 lds\n"
    "buffer_load_dwordx4 v15, s[44:47], s60 offen offset:3072 lds\n"
    "s_add_u32 m0, s62, 4096\n"
    "s_nop 0\n"
    "buffer_load_dwordx4 v16, s[44:47], s60 offen offset:0 lds\n"
    "s_setprio 0\n"
    "s_lshr_b32 s63, s36, 6\n"
    "s_add_u32 s63, s63, s87\n"
    "s_and_b32 s63, s63, 3\n"
    "s_lshl_b32 s63, s63, 2\n"
    "s_lshr_b32 s56, 0x43214321, s63\n"
    "s_and_b32 s56, s56, 0xffff\n"
    "s_sub_u32 s63, s87, 5\n"
    "s_cselect_b32 s63, s87, s63\n"
    "s_lshl_b32 s63, s63, 2\n"
    "s_mov_b32 s64, 0x21043210\n"
    "s_mov_b32 s65, 3\n"
    "s_lshr_b64 s[64:65], s[64:65], s63\n"
    "s_and_b32 s64, s64, 0xfffff\n"
    "s_lshl_b32 s63, s64, 16\n"
    "s_or_b32 s56, s56, s63\n"
    "s_lshr_b32 s57, s64, 16\n"
    "s_and_b32 s63, s56, 15\n"
    "s_lshl_b32 s64, s63, 8\n"
    "s_mul_i32 s63, s63, 20480\n"
    "s_add_u32 s66, s63, s39\n"
    "s_lshr_b64 s[56:57], s[56:57], 4\n"
    "s_add_u32 s65, s64, 128\n"
    "s_cbranch_vccz .Lc3%=\n"
    "s_waitcnt vmcnt(9)\n"
    "v_add_u32 v30, 1, v30\n"
    "s_add_u32 s63, s84, 13139968\n"
    "v_readfirstlane_b32 s76, v30\n"
    "s_mov_b64 exec, 1\n"
    "buffer_store_dword v30, off, s[52:55], s63 sc1\n"
    "s_mov_b64 exec, -1\n"
    ".Lc3%=:\n"
    "s_add_u32 m0, s38, 16384\n"
    "s_nop 0\n"
    "buffer_load_dwordx4 v10, s[40:43], s64 offen offset:0 lds\n"
    "buffer_load_dwordx4 v11, s[40:43], s64 offen offset:1024 lds\n"
    "s_add_u32 m0, s38, 24576\n"
    "s_and_b32 s63, s56, 15\n"
    "buffer_load_dwordx4 v10, s[40:43], s65 offen offset:0 lds\n"
    "buffer_load_dwordx4 v11, s[40:43], s65 offen offset:1024 lds\n"
    "s_mov_b32 m0, s66\n"
    "s_lshl_b32 s60, s63, 8\n"
    "buffer_load_dwordx4 v12, s[44:47], s64 offen offset:0 lds\n"
    "buffer_load_dwordx4 v13, s[44:47], s64 offen offset:1024 lds\n"
    "buffer_load_dwordx4 v14, s[44:47], s64 offen offset:2048 lds\n"
    "buffer_load_dwordx4 v15, s[44:47], s64 offen offset:3072 lds\n"
    "s_add_u32 m0, s66, 4096\n"
    "s_mul_i32 s63, s63, 20480\n"
    "buffer_load_dwordx4 v16, s[44:47], s64 offen offset:0 lds\n"
    "s_add_u32 s62, s63, s39\n"
    "s_lshr_b64 s[56:57], s[56:57], 4\n"
    "s_add_u32 s61, s60, 128\n"
    "s_cbranch_vccnz .Lp4%=\n"
    "s_waitcnt vmcnt(9)\n"
    ".Lp4%=:\n"
    "s_waitcnt vmcnt(10)\n"
    "s_barrier\n"
    "s_cbranch_vccz .Lc5%=\n"
    "v_lshlrev_b32 v24, 4, v1\n"
    "s_mov_b32 s85, 13139968\n"
    "s_mov_b32 s86, 13144064\n"
    ".Lc5%=:\n"
    "s_cbranch_vccz .Lc6%=\n"
    "s_cmp_lg_u32 s36, 192\n"
    "s_cbranch_scc1 .Lc7%=\n"
    "buffer_load_dwordx4 v[32:35], v24, s[52:55], s85 offen sc1\n"
    ".Lc7%=:\n"
    ".Lc6%=:\n"
    "s_lshr_b32 s87, s36, 3\n"
    "s_bfe_u32 s67, s36, 0x20001\n"
    "s_lshr_b32 s63, s87, 2\n"
    "s_add_u32 s63, s63, s87\n"
    "s_bitcmp1_b32 s63, 0\n"
    "s_mov_b32 s58, 0x86532410\n"
    "s_cselect_b32 s58, 0x96435210, s58\n"
    "s_mov_b32 s59, 0xfedbac97\n"
    "s_cselect_b32 s59, 0xfecbda87, s59\n"
    "s_mov_b32 s70, 0x75462310\n"
    "s_cselect_b32 s70, 0x65743210, s70\n"
    "s_mov_b32 s71, 0xfdceb98a\n"
    "s_cselect_b32 s71, 0xedfca98b, s71\n"
    "s_bitcmp1_b32 s63, 1\n"
    "s_cselect_b64 s[58:59], s[70:71], s[58:59]\n"
    "s_bitcmp1_b32 s36, 0\n"
    "s_cselect_b32 s70, 0x44444444, 0\n"
    "s_xor_b32 s58, s58, s70\n"
    "s_xor_b32 s59, s59, s70\n"
    "s_and_b32 s63, s87, 3\n"
    "s_lshl_b32 s69, s67, 2\n"
    "s_or_b32 s69, s69, s63\n"
    "s_cbranch_vccz .Lc8%=\n"
    "v_and_b32 v22, 15, v1\n"
    "s_lshl_b32 s70, s69, 10\n"
    "s_add_u32 s70, s70, 13107200\n"
    "v_lshl_add_u32 v22, v22, 6, s70\n"
    "v_add_u32 v23, 4, v22\n"
    "v_mov_b32 v40, 1\n"
    "s_add_u32 s84, s84, 13144064\n"
    "s_mov_b32 s78, 0\n"
    "s_lshl_b32 s77, s67, 6\n"
    "s_and_b32 s70, s87, 6\n"
    "s_lshl_b32 s70, s70, 3\n"
    "s_or_b32 s77, s77, s70\n"
    "s_and_b32 s70, s36, 1\n"
    "s_lshl_b32 s70, s70, 3\n"
    "s_or_b32 s77, s77, s70\n"
    "s_lshr_b32 s70, s87, 3\n"
    "s_lshl_b32 s70, s70, 1\n"
    "s_or_b32 s77, s77, s70\n"
    "s_and_b32 s70, s87, 1\n"
    "s_or_b32 s77, s77, s70\n"
    "s_lshl_b32 s77, s77, 6\n"
    "s_add_u32 s77, s77, 13107200\n"
    "s_bfe_u32 s70, s58, 0x30000\n"
    "s_lshl_b32 s70, s70, 1\n"
    "s_lshl_b32 s79, 3, s70\n"
    "s_bfe_u32 s70, s58, 0x30004\n"
    "s_lshl_b32 s70, s70, 1\n"
    "s_lshl_b32 s70, 3, s70\n"
    "s_or_b32 s79, s79, s70\n"
    "s_bfe_u32 s70, s58, 0x30008\n"
    "s_lshl_b32 s70, s70, 1\n"
    "s_lshl_b32 s80, 3, s70\n"
    "s_bfe_u32 s70, s58, 0x3000c\n"
    "s_lshl_b32 s70, s70, 1\n"
    "s_lshl_b32 s70, 3, s70\n"
    "s_or_b32 s80, s80, s70\n"
    ".Lc8%=:\n"
    "s_mul_i32 s69, s69, 327680\n"
    "s_and_b32 s63, s36, 1\n"
    "s_lshl_b32 s63, s63, 3\n"
    "s_lshr_b32 s70, s87, 2\n"
    "s_add_u32 s63, s63, s70\n"
    "s_lshl_b32 s63, s63, 1\n"
    "s_lshr_b32 s70, s37, 1\n"
    "s_add_u32 s63, s63, s70\n"
    "s_lshl_b32 s63, s63, 18\n"
    "s_and_b32 s70, s36, 2\n"
    "s_lshl_b32 s70, s70, 10\n"
    "s_add_u32 s63, s63, s70\n"
    "s_add_u32 s63, s63, 4096\n"
    "v_lshl_add_u32 v4, v2, 12, v3\n"
    "s_movk_i32 s70, 31744\n"
    "v_xad_u32 v5, v4, 64, s70\n"
    "s_lshl_b32 s70, s37, 1\n"
    "s_add_u32 s70, s70, s67\n"
    "s_and_b32 s71, s70, 3\n"
    "s_lshl_b32 s71, s71, 16\n"
    "s_add_u32 s71, s71, s63\n"
    "v_add_u32 v17, s71, v4\n"
    "v_add_u32 v18, s71, v5\n"
    "s_add_u32 s70, s70, 1\n"
    "s_and_b32 s70, s70, 3\n"
    "s_lshl_b32 s70, s70, 16\n"
    "s_add_u32 s70, s70, s63\n"
    "s_sub_u32 s70, s70, 2048\n"
    "v_add_u32 v19, s70, v4\n"
    "v_add_u32 v20, s70, v5\n"
    "s_sub_u32 s70, s68, 65536\n"
    "v_lshl_add_u32 v21, v1, 4, s70\n"
    "s_lshl_b32 s67, s37, 12\n"
    "s_lshr_b32 s70, s37, 1\n"
    "s_lshl_b32 s70, s70, 18\n"
    "s_sub_u32 s70, s63, s70\n"
    "s_and_b32 s71, s87, 3\n"
    "s_lshl_b32 s71, s71, 2\n"
    "s_add_u32 s71, s71, s37\n"
    "s_lshl_b32 s71, s71, 15\n"
    "s_add_u32 s70, s70, s71\n"
    "s_and_b32 s71, s36, 1\n"
    "s_lshl_b32 s71, s71, 9\n"
    "s_add_u32 s70, s70, s71\n"
    "v_and_b32 v6, 7, v1\n"
    "v_lshlrev_b32 v6, 12, v6\n"
    "v_and_b32 v25, 3, v2\n"
    "v_lshl_add_u32 v25, v25, 7, v6\n"
    "v_add_u32 v25, s70, v25\n"
    "s_sub_u32 s48, s48, 4096\n"
    "s_subb_u32 s49, s49, 0\n"
    "s_mov_b32 s50, 8392704\n"
    "s_mov_b32 s51, 0x20000\n"
    "s_add_u32 m0, s38, 32768\n"
    "s_nop 0\n"
    "buffer_load_dwordx4 v10, s[40:43], s60 offen offset:0 lds\n"
    "buffer_load_dwordx4 v11, s[40:43], s60 offen offset:1024 lds\n"
    "s_add_u32 m0, s38, 40960\n"
    "s_and_b32 s63, s56, 15\n"
    "buffer_load_dwordx4 v10, s[40:43], s61 offen offset:0 lds\n"
    "buffer_load_dwordx4 v11, s[40:43], s61 offen offset:1024 lds\n"
    "s_mov_b32 m0, s62\n"
    "s_lshl_b32 s64, s63, 8\n"
    "buffer_load_dwordx4 v12, s[44:47], s60 offen offset:0 lds\n"
    "buffer_load_dwordx4 v13, s[44:47], s60 offen offset:1024 lds\n"
    "buffer_load_dwordx4 v14, s[44:47], s60 offen offset:2048 lds\n"
    "buffer_load_dwordx4 v15, s[44:47], s60 offen offset:3072 lds\n"
    "s_add_u32 m0, s62, 4096\n"
    "s_mul_i32 s63, s63, 20480\n"
    "buffer_load_dwordx4 v16, s[44:47], s60 offen offset:0 lds\n"
    "s_add_u32 s66, s63, s39\n"
    "s_lshr_b64 s[56:57], s[56:57], 4\n"
    "s_add_u32 s65, s64, 128\n"
    "s_mov_b32 m0, s38\n"
    "s_cbranch_vccnz .Lp9%=\n"
    "s_waitcnt vmcnt(9)\n"
    ".Lp9%=:\n"
    "s_cmp_eq_u32 s36, 192\n"
    "s_cbranch_scc1 .Lq9%=\n"
    "s_waitcnt vmcnt(9)\n"
    ".Lq9%=:\n"
    "s_waitcnt vmcnt(10)\n"
    "s_barrier\n"
    "buffer_load_dwordx4 v10, s[40:43], s64 offen offset:0 lds\n"
    "buffer_load_dwordx4 v11, s[40:43], s64 offen offset:1024 lds\n"
    "s_add_u32 m0, s38, 8192\n"
    "s_and_b32 s63, s56, 15\n"
    "buffer_load_dwordx4 v10, s[40:43], s65 offen offset:0 lds\n"
    "buffer_load_dwordx4 v11, s[40:43], s65 offen offset:1024 lds\n"
    "s_mov_b32 m0, s66\n"
    "s_lshl_b32 s60, s63, 8\n"
    "buffer_load_dwordx4 v12, s[44:47], s64 offen offset:0 lds\n"
    "buffer_load_dwordx4 v13, s[44:47], s64 offen offset:1024 lds\n"
    "buffer_load_dwordx4 v14, s[44:47], s64 offen offset:2048 lds\n"
    "buffer_load_dwordx4 v15, s[44:47], s64 offen offset:3072 lds\n"
    "s_add_u32 m0, s66, 4096\n"
    "s_mul_i32 s63, s63, 20480\n"
    "buffer_load_dwordx4 v16, s[44:47], s64 offen offset:0 lds\n"
    "s_add_u32 s62, s63, s39\n"
    "s_lshr_b64 s[56:57], s[56:57], 4\n"
    "s_add_u32 s61, s60, 128\n"
    "s_cbranch_vccz .Lc10%=\n"
    "s_cmp_lg_u32 s36, 192\n"
    "s_cbranch_scc1 .Lc11%=\n"
    "s_waitcnt vmcnt(18)\n"
    "v_mov_b64 v[36:37], v[32:33]\n"
    "v_mov_b64 v[38:39], v[34:35]\n"
    "buffer_store_dwordx4 v[36:39], v24, s[52:55], s86 offen sc1\n"
    "s_nop 1\n"
    ".Le12%=:\n"
    "buffer_load_dwordx4 v[32:35], v24, s[52:55], s85 offen sc1\n"
    ".Lc11%=:\n"
    ".Lc10%=:\n"
    "s_add_u32 m0, s38, 16384\n"
    "s_cbranch_vccnz .Lp13%=\n"
    "s_waitcnt vmcnt(9)\n"
    ".Lp13%=:\n"
    "s_cmp_eq_u32 s36, 192\n"
    "s_cbranch_scc1 .Lq13%=\n"
    "s_waitcnt vmcnt(9)\n"
    ".Lq13%=:\n"
    "s_waitcnt vmcnt(10)\n"
    "s_barrier\n"
    "buffer_load_dwordx4 v10, s[40:43], s60 offen offset:0 lds\n"
    "buffer_load_dwordx4 v11, s[40:43], s60 offen offset:1024 lds\n"
    "s_add_u32 m0, s38, 24576\n"
    "s_and_b32 s63, s56, 15\n"
    "buffer_load_dwordx4 v10, s[40:43], s61 offen offset:0 lds\n"
    "buffer_load_dwordx4 v11, s[40:43], s61 offen offset:1024 lds\n"
    "s_mov_b32 m0, s62\n"
    "s_lshl_b32 s64, s63, 8\n"
    "buffer_load_dwordx4 v12, s[44:47], s60 offen offset:0 lds\n"
    "buffer_load_dwordx4 v13, s[44:47], s60 offen offset:1024 lds\n"
    "buffer_load_dwordx4 v14, s[44:47], s60 offen offset:2048 lds\n"
    "buffer_load_dwordx4 v15, s[44:47], s60 offen offset:3072 lds\n"
    "s_add_u32 m0, s62, 4096\n"
    "s_add_u32 s64, s64, 655360\n"
    "buffer_load_dwordx4 v16, s[44:47], s60 offen offset:0 lds\n"
    "s_lshr_b64 s[56:57], s[56:57], 4\n"
    "s_add_u32 s65, s64, 128\n"
    "s_cbranch_vccz .Lc14%=\n"
    "s_cmp_lg_u32 s36, 192\n"
    "s_cbranch_scc1 .Lc15%=\n"
    "s_waitcnt vmcnt(9)\n"
    "v_xor_b32 v41, v32, v36\n"
    "v_xor_b32 v42, v33, v37\n"
    "v_xor_b32 v43, v34, v38\n"
    "v_or3_b32 v41, v41, v42, v43\n"
    "v_xor_b32 v42, v35, v39\n"
    "v_or_b32 v41, v41, v42\n"
    "v_cmp_ne_u32 s[82:83], 0, v41\n"
    "s_nop 0\n"
    "s_cmp_eq_u64 s[82:83], 0\n"
    "s_cbranch_scc1 .Le16%=\n"
    "v_mov_b64 v[36:37], v[32:33]\n"
    "v_mov_b64 v[38:39], v[34:35]\n"
    "buffer_store_dwordx4 v[36:39], v24, s[52:55], s86 offen sc1\n"
    "s_nop 1\n"
    ".Le16%=:\n"
    "buffer_load_dwordx4 v[32:35], v24, s[52:55], s85 offen sc1\n"
    ".Lc15%=:\n"
    ".Lc14%=:\n"
    "s_add_u32 m0, s38, 32768\n"
    "s_cbranch_vccnz .Lp17%=\n"
    "s_waitcnt vmcnt(9)\n"
    ".Lp17%=:\n"
    "s_cmp_eq_u32 s36, 192\n"
    "s_cbranch_scc1 .Lq17%=\n"
    "s_waitcnt vmcnt(9)\n"
    ".Lq17%=:\n"
    "s_waitcnt vmcnt(11)\n"
    "s_barrier\n"
    "buffer_load_dwordx4 v10, s[40:43], s64 offen offset:0 lds\n"
    "buffer_load_dwordx4 v11, s[40:43], s64 offen offset:1024 lds\n"
    "s_add_u32 m0, s38, 40960\n"
    "s_and_b32 s63, s56, 15\n"
    "buffer_load_dwordx4 v10, s[40:43], s65 offen offset:0 lds\n"
    "buffer_load_dwordx4 v11, s[40:43], s65 offen offset:1024 lds\n"
    "s_lshl_b32 s60, s63, 8\n"
    "s_add_u32 s60, s60, 655360\n"
    "s_lshr_b64 s[56:57], s[56:57], 4\n"
    "s_add_u32 s61, s60, 128\n"
    "s_mov_b32 m0, s38\n"
    "s_cbranch_vccnz .Lp18%=\n"
    "s_waitcnt vmcnt(4)\n"
    ".Lp18%=:\n"
    "s_cmp_eq_u32 s36, 192\n"
    "s_cbranch_scc1 .Lq18%=\n"
    "s_waitcnt vmcnt(4)\n"
    ".Lq18%=:\n"
    "s_waitcnt vmcnt(5)\n"
    "s_barrier\n"
    "buffer_load_dwordx4 v10, s[40:43], s60 offen offset:0 lds\n"
    "buffer_load_dwordx4 v11, s[40:43], s60 offen offset:1024 lds\n"
    "s_add_u32 m0, s38, 8192\n"
    "s_and_b32 s63, s56, 15\n"
    "buffer_load_dwordx4 v10, s[40:43], s61 offen offset:0 lds\n"
    "buffer_load_dwordx4 v11, s[40:43], s61 offen offset:1024 lds\n"
    "s_lshl_b32 s64, s63, 8\n"
    "s_add_u32 s64, s64, 655360\n"
    "s_lshr_b64 s[56:57], s[56:57], 4\n"
    "s_add_u32 s65, s64, 128\n"
    "s_cbranch_vccz .Lc19%=\n"
    "s_cmp_lg_u32 s36, 192\n"
    "s_cbranch_scc1 .Lc20%=\n"
    "s_waitcnt vmcnt(8)\n"
    "v_xor_b32 v41, v32, v36\n"
    "v_xor_b32 v42, v33, v37\n"
    "v_xor_b32 v43, v34, v38\n"
    "v_or3_b32 v41, v41, v42, v43\n"
    "v_xor_b32 v42, v35, v39\n"
    "v_or_b32 v41, v41, v42\n"
    "v_cmp_ne_u32 s[82:83], 0, v41\n"
    "s_nop 0\n"
    "s_cmp_eq_u64 s[82:83], 0\n"
    "s_cbranch_scc1 .Le21%=\n"
    "v_mov_b64 v[36:37], v[32:33]\n"
    "v_mov_b64 v[38:39], v[34:35]\n"
    "buffer_store_dwordx4 v[36:39], v24, s[52:55], s86 offen sc1\n"
    "s_nop 1\n"
    ".Le21%=:\n"
    "buffer_load_dwordx4 v[32:35], v24, s[52:55], s85 offen sc1\n"
    ".Lc20%=:\n"
    ".Lc19%=:\n"
    "s_add_u32 m0, s38, 16384\n"
    "s_cbranch_vccnz .Lp22%=\n"
    "s_waitcnt vmcnt(4)\n"
    ".Lp22%=:\n"
    "s_cmp_eq_u32 s36, 192\n"
    "s_cbranch_scc1 .Lq22%=\n"
    "s_waitcnt vmcnt(4)\n"
    ".Lq22%=:\n"
    "s_waitcnt vmcnt(5)\n"
    "s_barrier\n"
    "buffer_load_dwordx4 v10, s[40:43], s64 offen offset:0 lds\n"
    "buffer_load_dwordx4 v11, s[40:43], s64 offen offset:1024 lds\n"
    "s_add_u32 m0, s38, 24576\n"
    "s_and_b32 s63, s56, 15\n"
    "buffer_load_dwordx4 v10, s[40:43], s65 offen offset:0 lds\n"
    "buffer_load_dwordx4 v11, s[40:43], s65 offen offset:1024 lds\n"
    "s_lshl_b32 s60, s63, 8\n"
    "s_add_u32 s60, s60, 655360\n"
    "s_lshr_b64 s[56:57], s[56:57], 4\n"
    "s_add_u32 s61, s60, 128\n"
    "s_cbranch_vccz .Lc23%=\n"
    "buffer_load_dword v31, off, s[52:55], s84 sc1\n"
    ".Lc23%=:\n"
    "s_add_u32 m0, s38, 32768\n"
    "s_cbranch_vccnz .Lp24%=\n"
    "s_waitcnt vmcnt(4)\n"
    ".Lp24%=:\n"
    "s_cmp_eq_u32 s36, 192\n"
    "s_cbranch_scc1 .Lq24%=\n"
    "s_waitcnt vmcnt(5)\n"
    ".Lq24%=:\n"
    "s_waitcnt vmcnt(6)\n"
    "s_barrier\n"
    "buffer_load_dwordx4 v10, s[40:43], s60 offen offset:0 lds\n"
    "buffer_load_dwordx4 v11, s[40:43], s60 offen offset:1024 lds\n"
    "s_add_u32 m0, s38, 40960\n"
    "s_and_b32 s63, s56, 15\n"
    "buffer_load_dwordx4 v10, s[40:43], s61 offen offset:0 lds\n"
    "buffer_load_dwordx4 v11, s[40:43], s61 offen offset:1024 lds\n"
    "s_lshl_b32 s64, s63, 8\n"
    "s_add_u32 s64, s64, 655360\n"
    "s_lshr_b64 s[56:57], s[56:57], 4\n"
    "s_add_u32 s65, s64, 128\n"
    "s_cbranch_vccz .Lc25%=\n"
    "s_cmp_lg_u32 s36, 192\n"
    "s_cbranch_scc1 .Lc26%=\n"
    "s_waitcnt vmcnt(9)\n"
    "v_xor_b32 v41, v32, v36\n"
    "v_xor_b32 v42, v33, v37\n"
    "v_xor_b32 v43, v34, v38\n"
    "v_or3_b32 v41, v41, v42, v43\n"
    "v_xor_b32 v42, v35, v39\n"
    "v_or_b32 v41, v41, v42\n"
    "v_cmp_ne_u32 s[82:83], 0, v41\n"
    "s_nop 0\n"
    "s_cmp_eq_u64 s[82:83], 0\n"
    "s_cbranch_scc1 .Le27%=\n"
    "v_mov_b64 v[36:37], v[32:33]\n"
    "v_mov_b64 v[38:39], v[34:35]\n"
    "buffer_store_dwordx4 v[36:39], v24, s[52:55], s86 offen sc1\n"
    "s_nop 1\n"
    ".Le27%=:\n"
    "buffer_load_dwordx4 v[32:35], v24, s[52:55], s85 offen sc1\n"
    ".Lc26%=:\n"
    ".Lc25%=:\n"
    "s_mov_b32 m0, s38\n"
    "s_cbranch_vccnz .Lp28%=\n"
    "s_waitcnt vmcnt(4)\n"
    ".Lp28%=:\n"
    "s_cmp_eq_u32 s36, 192\n"
    "s_cbranch_scc1 .Lq28%=\n"
    "s_waitcnt vmcnt(5)\n"
    ".Lq28%=:\n"
    "s_waitcnt vmcnt(6)\n"
    "s_barrier\n"
    "s_cbranch_vccz .Lc29%=\n"
    "s_waitcnt vmcnt(4)\n"
    "s_mov_b32 s81, 65536\n"
    ".Lh30%=:\n"
    "v_readfirstlane_b32 s63, v31\n"
    "s_nop 0\n"
    "s_cmp_eq_u32 s63, s76\n"
    "s_cbranch_scc1 .Lg30%=\n"
    "s_sub_u32 s81, s81, 1\n"
    "s_cbranch_scc1 .Lg30%=\n"
    "s_cmp_lg_u32 s36, 192\n"
    "s_cbranch_scc1 .Lc31%=\n"
    "buffer_load_dwordx4 v[44:47], v24, s[52:55], s85 offen sc1\n"
    "s_waitcnt vmcnt(0)\n"
    "buffer_store_dwordx4 v[44:47], v24, s[52:55], s86 offen sc1\n"
    ".Lc31%=:\n"
    "buffer_load_dword v31, off, s[52:55], s84 sc1\n"
    "s_waitcnt vmcnt(0)\n"
    "s_branch .Lh30%=\n"
    ".Lg30%=:\n"
    "s_mov_b64 exec, 1\n"
    "buffer_store_dword v40, off, s[52:55], s77 sc1\n"
    "s_mov_b64 exec, -1\n"
    ".Lc29%=:\n"
    "buffer_load_dwordx4 v10, s[40:43], s64 offen offset:0 lds\n"
    "buffer_load_dwordx4 v11, s[40:43], s64 offen offset:1024 lds\n"
    "s_add_u32 m0, s38, 8192\n"
    "s_nop 0\n"
    "buffer_load_dwordx4 v10, s[40:43], s65 offen offset:0 lds\n"
    "buffer_load_dwordx4 v11, s[40:43], s65 offen offset:1024 lds\n"
    "s_mov_b32 m0, 152320\n"
    "s_nop 0\n"
    "buffer_load_dword v25, s[48:51], 0 offen lds\n"
    "s_and_b32 s63, s58, 15\n"
    "s_lshl_b32 s70, s63, 7\n"
    "s_mul_i32 s63, s63, 20480\n"
    "s_add_u32 s71, s63, s69\n"
    "s_lshr_b64 s[58:59], s[58:59], 4\n"
    "s_add_u32 s72, s71, 4096\n"
    "s_and_b32 s63, s58, 15\n"
    "s_lshl_b32 s73, s63, 7\n"
    "s_mul_i32 s63, s63, 20480\n"
    "s_add_u32 s74, s63, s69\n"
    "s_lshr_b64 s[58:59], s[58:59], 4\n"
    "s_add_u32 s75, s74, 4096\n"
    "s_add_u32 m0, s67, 16384\n"
    "s_cbranch_vccnz .Lp32%=\n"
    "s_waitcnt vmcnt(5)\n"
    ".Lp32%=:\n"
    "s_cmp_eq_u32 s36, 192\n"
    "s_cbranch_scc1 .Lq32%=\n"
    "s_waitcnt vmcnt(6)\n"
    ".Lq32%=:\n"
    "s_waitcnt vmcnt(7)\n"
    "s_barrier\n"
    "buffer_load_dwordx4 v17, s[48:51], s70 offen offset:0 lds\n"
    "buffer_load_dwordx4 v18, s[48:51], s70 offen offset:1024 lds\n"
    "buffer_load_dwordx4 v19, s[48:51], s70 offen offset:2048 lds\n"
    "buffer_load_dwordx4 v20, s[48:51], s70 offen offset:3072 lds\n"
    "s_add_u32 m0, s67, 32768\n"
    "s_waitcnt vmcnt(5)\n"
    "s_barrier\n"
    "s_sleep 3\n"
    "s_cbranch_vccz .Ls33%=\n"
    "buffer_load_dword v31, v22, s[52:55], 0 offen sc1\n"
    "s_branch .Lt33%=\n"
    ".Ls33%=:\n"
    "s_sleep 1\n"
    ".Lt33%=:\n"
    "buffer_load_dwordx4 v17, s[48:51], s73 offen offset:0 lds\n"
    "buffer_load_dwordx4 v18, s[48:51], s73 offen offset:1024 lds\n"
    "buffer_load_dwordx4 v19, s[48:51], s73 offen offset:2048 lds\n"
    "buffer_load_dwordx4 v20, s[48:51], s73 offen offset:3072 lds\n"
    "s_mov_b32 m0, s68\n"
    "s_cbranch_vccz .Lc34%=\n"
    "s_waitcnt vmcnt(4)\n"
    "v_cmp_eq_u32 s[82:83], 1, v31\n"
    "s_nop 0\n"
    "s_mov_b32 s78, s82\n"
    "s_mov_b32 s81, 65536\n"
    ".Ln35%=:\n"
    "s_and_b32 s63, s78, s79\n"
    "s_cmp_eq_u32 s63, s79\n"
    "s_cbranch_scc1 .Lm35%=\n"
    "buffer_load_dword v31, v22, s[52:55], 0 offen sc1\n"
    "s_waitcnt vmcnt(0)\n"
    "v_cmp_eq_u32 s[82:83], 1, v31\n"
    "s_sub_u32 s81, s81, 1\n"
    "s_cbranch_scc1 .Lm35%=\n"
    "s_mov_b32 s78, s82\n"
    "s_and_b32 s63, s78, s79\n"
    "s_cmp_eq_u32 s63, s79\n"
    "s_cbranch_scc1 .Lm35%=\n"
    "s_cmp_lg_u32 s36, 192\n"
    "s_cbranch_scc1 .Lc36%=\n"
    "buffer_load_dwordx4 v[44:47], v24, s[52:55], s85 offen sc1\n"
    "s_waitcnt vmcnt(0)\n"
    "buffer_store_dwordx4 v[44:47], v24, s[52:55], s86 offen sc1\n"
    ".Lc36%=:\n"
    "s_sleep 1\n"
    "s_branch .Ln35%=\n"
    ".Lm35%=:\n"
    ".Lc34%=:\n"
    "s_barrier\n"
    "s_cbranch_vccz .Lc37%=\n"
    "buffer_load_dword v31, v22, s[52:55], 0 offen sc1\n"
    ".Lc37%=:\n"
    "buffer_load_dwordx4 v21, s[52:55], s71 offen offset:0 sc1 lds\n"
    "buffer_load_dwordx4 v21, s[52:55], s71 offen offset:1024 sc1 lds\n"
    "buffer_load_dwordx4 v21, s[52:55], s71 offen offset:2048 sc1 lds\n"
    "buffer_load_dwordx4 v21, s[52:55], s71 offen offset:3072 sc1 lds\n"
    "s_add_u32 m0, s68, 4096\n"
    "s_nop 0\n"
    "buffer_load_dwordx4 v21, s[52:55], s72 offen offset:0 sc1 lds\n"
    "s_add_u32 m0, s68, 20480\n"
    "s_nop 0\n"
    "buffer_load_dwordx4 v21, s[52:55], s74 offen offset:0 sc1 lds\n"
    "buffer_load_dwordx4 v21, s[52:55], s74 offen offset:1024 sc1 lds\n"
    "buffer_load_dwordx4 v21, s[52:55], s74 offen offset:2048 sc1 lds\n"
    "buffer_load_dwordx4 v21, s[52:55], s74 offen offset:3072 sc1 lds\n"
    "s_add_u32 m0, s68, 24576\n"
    "s_nop 0\n"
    "buffer_load_dwordx4 v21, s[52:55], s75 offen offset:0 sc1 lds\n"
    "s_and_b32 s63, s58, 15\n"
    "s_lshl_b32 s70, s63, 7\n"
    "s_mul_i32 s63, s63, 20480\n"
    "s_add_u32 s71, s63, s69\n"
    "s_lshr_b64 s[58:59], s[58:59], 4\n"
    "s_add_u32 s72, s71, 4096\n"
    "s_add_u32 m0, s68, 40960\n"
    "s_waitcnt vmcnt(5)\n"
    "s_cbranch_vccz .Lc38%=\n"
    "v_cmp_eq_u32 s[82:83], 1, v31\n"
    "s_nop 0\n"
    "s_or_b32 s78, s78, s82\n"
    "s_mov_b32 s81, 65536\n"
    ".Ln39%=:\n"
    "s_and_b32 s63, s78, s80\n"
    "s_cmp_eq_u32 s63, s80\n"
    "s_cbranch_scc1 .Lm39%=\n"
    "buffer_load_dword v31, v22, s[52:55], 0 offen sc1\n"
    "s_waitcnt vmcnt(0)\n"
    "v_cmp_eq_u32 s[82:83], 1, v31\n"
    "s_sub_u32 s81, s81, 1\n"
    "s_cbranch_scc1 .Lm39%=\n"
    "s_mov_b32 s78, s82\n"
    "s_and_b32 s63, s78, s80\n"
    "s_cmp_eq_u32 s63, s80\n"
    "s_cbranch_scc1 .Lm39%=\n"
    "s_cmp_lg_u32 s36, 192\n"
    "s_cbranch_scc1 .Lc40%=\n"
    "buffer_load_dwordx4 v[44:47], v24, s[52:55], s85 offen sc1\n"
    "s_waitcnt vmcnt(0)\n"
    "buffer_store_dwordx4 v[44:47], v24, s[52:55], s86 offen sc1\n"
    ".Lc40%=:\n"
    "s_sleep 1\n"
    "s_branch .Ln39%=\n"
    ".Lm39%=:\n"
    ".Lc38%=:\n"
    "s_barrier\n"
    "s_cbranch_vccz .Lc41%=\n"
    "s_mov_b32 s81, 65536\n"
    ".Ln42%=:\n"
    "s_and_b32 s63, s78, 0xffff\n"
    "s_cmp_eq_u32 s63, 0xffff\n"
    "s_cbranch_scc1 .Lm42%=\n"
    "buffer_load_dword v31, v22, s[52:55], 0 offen sc1\n"
    "s_waitcnt vmcnt(0)\n"
    "v_cmp_eq_u32 s[82:83], 1, v31\n"
    "s_sub_u32 s81, s81, 1\n"
    "s_cbranch_scc1 .Lm42%=\n"
    "s_mov_b32 s78, s82\n"
    "s_and_b32 s63, s78, 0xffff\n"
    "s_cmp_eq_u32 s63, 0xffff\n"
    "s_cbranch_scc1 .Lm42%=\n"
    "s_cmp_lg_u32 s36, 192\n"
    "s_cbranch_scc1 .Lc43%=\n"
    "buffer_load_dwordx4 v[44:47], v24, s[52:55], s85 offen sc1\n"
    "s_waitcnt vmcnt(0)\n"
    "buffer_store_dwordx4 v[44:47], v24, s[52:55], s86 offen sc1\n"
    ".Lc43%=:\n"
    "s_sleep 1\n"
    "s_branch .Ln42%=\n"
    ".Lm42%=:\n"
    ".Lc41%=:\n"
    "buffer_load_dwordx4 v21, s[52:55], s71 offen offset:0 sc1 lds\n"
    "buffer_load_dwordx4 v21, s[52:55], s71 offen offset:1024 sc1 lds\n"
    "s_and_b32 s63, s58, 15\n"
    "buffer_load_dwordx4 v21, s[52:55], s71 offen offset:2048 sc1 lds\n"
    "buffer_load_dwordx4 v21, s[52:55], s71 offen offset:3072 sc1 lds\n"
    "s_lshl_b32 s73, s63, 7\n"
    "s_add_u32 m0, s68, 45056\n"
    "s_mul_i32 s63, s63, 20480\n"
    "buffer_load_dwordx4 v21, s[52:55], s72 offen offset:0 sc1 lds\n"
    "s_add_u32 m0, s67, 0\n"
    "s_add_u32 s74, s63, s69\n"
    "buffer_load_dwordx4 v17, s[48:51], s70 offen offset:0 lds\n"
    "s_lshr_b64 s[58:59], s[58:59], 4\n"
    "buffer_load_dwordx4 v18, s[48:51], s70 offen offset:1024 lds\n"
    "buffer_load_dwordx4 v19, s[48:51], s70 offen offset:2048 lds\n"
    "s_add_u32 s75, s74, 4096\n"
    "buffer_load_dwordx4 v20, s[48:51], s70 offen offset:3072 lds\n"
    "s_add_u32 m0, s68, 61440\n"
    "s_waitcnt vmcnt(9)\n"
    "s_barrier\n"
    "s_cbranch_vccz .Lc44%=\n"
    "s_mov_b64 exec, 1\n"
    "buffer_store_dword v40, off, s[52:55], s77 offset:4 sc1\n"
    "s_mov_b64 exec, -1\n"
    ".Lc44%=:\n"
    "buffer_load_dwordx4 v21, s[52:55], s74 offen offset:0 sc1 lds\n"
    "buffer_load_dwordx4 v21, s[52:55], s74 offen offset:1024 sc1 lds\n"
    "buffer_load_dwordx4 v21, s[52:55], s74 offen offset:2048 sc1 lds\n"
    "s_and_b32 s63, s58, 15\n"
    "buffer_load_dwordx4 v21, s[52:55], s74 offen offset:3072 sc1 lds\n"
    "s_add_u32 m0, s68, 65536\n"
    "s_lshl_b32 s70, s63, 7\n"
    "buffer_load_dwordx4 v21, s[52:55], s75 offen offset:0 sc1 lds\n"
    "s_mul_i32 s63, s63, 20480\n"
    "s_add_u32 m0, s67, 49152\n"
    "s_add_u32 s71, s63, s69\n"
    "buffer_load_dwordx4 v17, s[48:51], s73 offen offset:0 lds\n"
    "buffer_load_dwordx4 v18, s[48:51], s73 offen offset:1024 lds\n"
    "s_lshr_b64 s[58:59], s[58:59], 4\n"
    "buffer_load_dwordx4 v19, s[48:51], s73 offen offset:2048 lds\n"
    "buffer_load_dwordx4 v20, s[48:51], s73 offen offset:3072 lds\n"
    "s_add_u32 s72, s71, 4096\n"
    "s_add_u32 m0, s68, 0\n"
    "s_cbranch_vccz .Lc45%=\n"
    "s_cmp_lg_u32 s36, 192\n"
    "s_cbranch_scc1 .Lc46%=\n"
    "s_waitcnt vmcnt(45)\n"
    "v_xor_b32 v41, v32, v36\n"
    "v_xor_b32 v42, v33, v37\n"
    "v_xor_b32 v43, v34, v38\n"
    "v_or3_b32 v41, v41, v42, v43\n"
    "v_xor_b32 v42, v35, v39\n"
    "v_or_b32 v41, v41, v42\n"
    "v_cmp_ne_u32 s[82:83], 0, v41\n"
    "s_nop 0\n"
    "s_cmp_eq_u64 s[82:83], 0\n"
    "s_cbranch_scc1 .Le47%=\n"
    "v_mov_b64 v[36:37], v[32:33]\n"
    "v_mov_b64 v[38:39], v[34:35]\n"
    "buffer_store_dwordx4 v[36:39], v24, s[52:55], s86 offen sc1\n"
    "s_nop 1\n"
    ".Le47%=:\n"
    "buffer_load_dwordx4 v[32:35], v24, s[52:55], s85 offen sc1\n"
    ".Lc46%=:\n"
    ".Lc45%=:\n"
    "s_cbranch_vccnz .Lp48%=\n"
    "s_waitcnt vmcnt(9)\n"
    ".Lp48%=:\n"
    "s_cmp_eq_u32 s36, 192\n"
    "s_cbranch_scc1 .Lq48%=\n"
    "s_waitcnt vmcnt(10)\n"
    ".Lq48%=:\n"
    "s_waitcnt vmcnt(11)\n"
    "s_barrier\n"
    "buffer_load_dwordx4 v21, s[52:55], s71 offen offset:0 sc1 lds\n"
    "buffer_load_dwordx4 v21, s[52:55], s71 offen offset:1024 sc1 lds\n"
    "s_and_b32 s63, s58, 15\n"
    "buffer_load_dwordx4 v21, s[52:55], s71 offen offset:2048 sc1 lds\n"
    "buffer_load_dwordx4 v21, s[52:55], s71 offen offset:3072 sc1 lds\n"
    "s_lshl_b32 s73, s63, 7\n"
    "s_add_u32 m0, s68, 4096\n"
    "s_mul_i32 s63, s63, 20480\n"
    "buffer_load_dwordx4 v21, s[52:55], s72 offen offset:0 sc1 lds\n"
    "s_add_u32 m0, s67, 16384\n"
    "s_add_u32 s74, s63, s69\n"
    "buffer_load_dwordx4 v17, s[48:51], s70 offen offset:0 lds\n"
    "s_lshr_b64 s[58:59], s[58:59], 4\n"
    "buffer_load_dwordx4 v18, s[48:51], s70 offen offset:1024 lds\n"
    "buffer_load_dwordx4 v19, s[48:51], s70 offen offset:2048 lds\n"
    "s_add_u32 s75, s74, 4096\n"
    "buffer_load_dwordx4 v20, s[48:51], s70 offen offset:3072 lds\n"
    "s_add_u32 m0, s68, 20480\n"
    "s_cbranch_vccnz .Lp49%=\n"
    "s_waitcnt vmcnt(9)\n"
    ".Lp49%=:\n"
    "s_cmp_eq_u32 s36, 192\n"
    "s_cbranch_scc1 .Lq49%=\n"
    "s_waitcnt vmcnt(9)\n"
    ".Lq49%=:\n"
    "s_waitcnt vmcnt(10)\n"
    "s_barrier\n"
    "buffer_load_dwordx4 v21, s[52:55], s74 offen offset:0 sc1 lds\n"
    "buffer_load_dwordx4 v21, s[52:55], s74 offen offset:1024 sc1 lds\n"
    "buffer_load_dwordx4 v21, s[52:55], s74 offen offset:2048 sc1 lds\n"
    "s_and_b32 s63, s58, 15\n"
    "buffer_load_dwordx4 v21, s[52:55], s74 offen offset:3072 sc1 lds\n"
    "s_add_u32 m0, s68, 24576\n"
    "s_lshl_b32 s70, s63, 7\n"
    "buffer_load_dwordx4 v21, s[52:55], s75 offen offset:0 sc1 lds\n"
    "s_mul_i32 s63, s63, 20480\n"
    "s_add_u32 m0, s67, 32768\n"
    "s_add_u32 s71, s63, s69\n"
    "buffer_load_dwordx4 v17, s[48:51], s73 offen offset:0 lds\n"
    "buffer_load_dwordx4 v18, s[48:51], s73 offen offset:1024 lds\n"
    "s_lshr_b64 s[58:59], s[58:59], 4\n"
    "buffer_load_dwordx4 v19, s[48:51], s73 offen offset:2048 lds\n"
    "buffer_load_dwordx4 v20, s[48:51], s73 offen offset:3072 lds\n"
    "s_add_u32 s72, s71, 4096\n"
    "s_add_u32 m0, s68, 40960\n"
    "s_cbranch_vccz .Lc50%=\n"
    "buffer_load_dword v31, v23, s[52:55], 0 offen sc1\n"
    ".Lc50%=:\n"
    "s_cbranch_vccnz .Lp51%=\n"
    "s_waitcnt vmcnt(9)\n"
    ".Lp51%=:\n"
    "s_waitcnt vmcnt(10)\n"
    "s_barrier\n"
    "buffer_load_dwordx4 v21, s[52:55], s71 offen offset:0 sc1 lds\n"
    "buffer_load_dwordx4 v21, s[52:55], s71 offen offset:1024 sc1 lds\n"
    "s_and_b32 s63, s58, 15\n"
    "buffer_load_dwordx4 v21, s[52:55], s71 offen offset:2048 sc1 lds\n"
    "buffer_load_dwordx4 v21, s[52:55], s71 offen offset:3072 sc1 lds\n"
    "s_lshl_b32 s73, s63, 7\n"
    "s_add_u32 m0, s68, 45056\n"
    "s_mul_i32 s63, s63, 20480\n"
    "buffer_load_dwordx4 v21, s[52:55], s72 offen offset:0 sc1 lds\n"
    "s_add_u32 m0, s67, 0\n"
    "s_add_u32 s74, s63, s69\n"
    "buffer_load_dwordx4 v17, s[48:51], s70 offen offset:0 lds\n"
    "s_lshr_b64 s[58:59], s[58:59], 4\n"
    "buffer_load_dwordx4 v18, s[48:51], s70 offen offset:1024 lds\n"
    "buffer_load_dwordx4 v19, s[48:51], s70 offen offset:2048 lds\n"
    "s_add_u32 s75, s74, 4096\n"
    "buffer_load_dwordx4 v20, s[48:51], s70 offen offset:3072 lds\n"
    "s_add_u32 m0, s68, 61440\n"
    "s_cbranch_vccnz .Lp52%=\n"
    "s_waitcnt vmcnt(9)\n"
    ".Lp52%=:\n"
    "s_waitcnt vmcnt(10)\n"
    "s_cbranch_vccz .Lc53%=\n"
    "s_waitcnt vmcnt(9)\n"
    "s_mov_b32 s81, 65536\n"
    ".Lck%=:\n"
    "v_cmp_ne_u32 s[82:83], 1, v31\n"
    "s_nop 0\n"
    "s_cmp_eq_u64 s[82:83], 0\n"
    "s_cbranch_scc1 .Lok%=\n"
    "s_sub_u32 s81, s81, 1\n"
    "s_cbranch_scc1 .Lok%=\n"
    "s_cmp_lg_u32 s36, 192\n"
    "s_cbranch_scc1 .Lc54%=\n"
    "buffer_load_dwordx4 v[44:47], v24, s[52:55], s85 offen sc1\n"
    "s_waitcnt vmcnt(0)\n"
    "buffer_store_dwordx4 v[44:47], v24, s[52:55], s86 offen sc1\n"
    ".Lc54%=:\n"
    "s_sleep 1\n"
    "buffer_load_dword v31, v23, s[52:55], 0 offen sc1\n"
    "s_waitcnt vmcnt(0)\n"
    "s_branch .Lck%=\n"
    ".Lok%=:\n"
    ".Lc53%=:\n"
    "s_barrier\n"
    "buffer_load_dwordx4 v21, s[52:55], s74 offen offset:0 sc1 lds\n"
    "buffer_load_dwordx4 v21, s[52:55], s74 offen offset:1024 sc1 lds\n"
    "buffer_load_dwordx4 v21, s[52:55], s74 offen offset:2048 sc1 lds\n"
    "s_and_b32 s63, s58, 15\n"
    "buffer_load_dwordx4 v21, s[52:55], s74 offen offset:3072 sc1 lds\n"
    "s_add_u32 m0, s68, 65536\n"
    "s_lshl_b32 s70, s63, 7\n"
    "buffer_load_dwordx4 v21, s[52:55], s75 offen offset:0 sc1 lds\n"
    "s_mul_i32 s63, s63, 20480\n"
    "s_add_u32 m0, s67, 49152\n"
    "s_add_u32 s71, s63, s69\n"
    "buffer_load_dwordx4 v17, s[48:51], s73 offen offset:0 lds\n"
    "buffer_load_dwordx4 v18, s[48:51], s73 offen offset:1024 lds\n"
    "s_lshr_b64 s[58:59], s[58:59], 4\n"
    "buffer_load_dwordx4 v19, s[48:51], s73 offen offset:2048 lds\n"
    "buffer_load_dwordx4 v20, s[48:51], s73 offen offset:3072 lds\n"
    "s_add_u32 s72, s71, 4096\n"
    "s_add_u32 m0, s68, 0\n"
    "s_nop 0\n"
    "buffer_load_dwordx4 v21, s[52:55], s71 offen offset:0 sc1 lds\n"
    "s_cbranch_vccz .Lc55%=\n"
    "s_cmp_lg_u32 s36, 192\n"
    "s_cbranch_scc1 .Lc56%=\n"
    "s_waitcnt vmcnt(38)\n"
    "v_xor_b32 v41, v32, v36\n"
    "v_xor_b32 v42, v33, v37\n"
    "v_xor_b32 v43, v34, v38\n"
    "v_or3_b32 v41, v41, v42, v43\n"
    "v_xor_b32 v42, v35, v39\n"
    "v_or_b32 v41, v41, v42\n"
    "v_cmp_ne_u32 s[82:83], 0, v41\n"
    "s_nop 0\n"
    "s_cmp_eq_u64 s[82:83], 0\n"
    "s_cbranch_scc1 .Le57%=\n"
    "v_mov_b64 v[36:37], v[32:33]\n"
    "v_mov_b64 v[38:39], v[34:35]\n"
    "buffer_store_dwordx4 v[36:39], v24, s[52:55], s86 offen sc1\n"
    "s_nop 1\n"
    ".Le57%=:\n"
    "buffer_load_dwordx4 v[32:35], v24, s[52:55], s85 offen sc1\n"
    ".Lc56%=:\n"
    ".Lc55%=:\n"
    "s_cbranch_vccnz .Lp58%=\n"
    "s_waitcnt vmcnt(10)\n"
    ".Lp58%=:\n"
    "s_cmp_eq_u32 s36, 192\n"
    "s_cbranch_scc1 .Lq58%=\n"
    "s_waitcnt vmcnt(10)\n"
    ".Lq58%=:\n"
    "s_waitcnt vmcnt(11)\n"
    "s_barrier\n"
    "buffer_load_dwordx4 v21, s[52:55], s71 offen offset:1024 sc1 lds\n"
    "s_and_b32 s63, s58, 15\n"
    "buffer_load_dwordx4 v21, s[52:55], s71 offen offset:2048 sc1 lds\n"
    "buffer_load_dwordx4 v21, s[52:55], s71 offen offset:3072 sc1 lds\n"
    "s_lshl_b32 s73, s63, 7\n"
    "s_add_u32 m0, s68, 4096\n"
    "s_mul_i32 s63, s63, 20480\n"
    "buffer_load_dwordx4 v21, s[52:55], s72 offen offset:0 sc1 lds\n"
    "s_add_u32 m0, s67, 16384\n"
    "s_add_u32 s74, s63, s69\n"
    "buffer_load_dwordx4 v17, s[48:51], s70 offen offset:0 lds\n"
    "s_lshr_b64 s[58:59], s[58:59], 4\n"
    "buffer_load_dwordx4 v18, s[48:51], s70 offen offset:1024 lds\n"
    "buffer_load_dwordx4 v19, s[48:51], s70 offen offset:2048 lds\n"
    "s_add_u32 s75, s74, 4096\n"
    "buffer_load_dwordx4 v20, s[48:51], s70 offen offset:3072 lds\n"
    "s_add_u32 m0, s68, 20480\n"
    "s_nop 0\n"
    "buffer_load_dwordx4 v21, s[52:55], s74 offen offset:0 sc1 lds\n"
    "buffer_load_dwordx4 v21, s[52:55], s74 offen offset:1024 sc1 lds\n"
    "s_cbranch_vccnz .Lp59%=\n"
    "s_waitcnt vmcnt(11)\n"
    ".Lp59%=:\n"
    "s_cmp_eq_u32 s36, 192\n"
    "s_cbranch_scc1 .Lq59%=\n"
    "s_waitcnt vmcnt(11)\n"
    ".Lq59%=:\n"
    "s_waitcnt vmcnt(12)\n"
    "s_barrier\n"
    "buffer_load_dwordx4 v21, s[52:55], s74 offen offset:2048 sc1 lds\n"
    "s_and_b32 s63, s58, 15\n"
    "buffer_load_dwordx4 v21, s[52:55], s74 offen offset:3072 sc1 lds\n"
    "s_add_u32 m0, s68, 24576\n"
    "s_lshl_b32 s70, s63, 7\n"
    "buffer_load_dwordx4 v21, s[52:55], s75 offen offset:0 sc1 lds\n"
    "s_mul_i32 s63, s63, 20480\n"
    "s_add_u32 m0, s67, 32768\n"
    "s_add_u32 s71, s63, s69\n"
    "buffer_load_dwordx4 v17, s[48:51], s73 offen offset:0 lds\n"
    "buffer_load_dwordx4 v18, s[48:51], s73 offen offset:1024 lds\n"
    "s_lshr_b64 s[58:59], s[58:59], 4\n"
    "buffer_load_dwordx4 v19, s[48:51], s73 offen offset:2048 lds\n"
    "buffer_load_dwordx4 v20, s[48:51], s73 offen offset:3072 lds\n"
    "s_add_u32 s72, s71, 4096\n"
    "s_add_u32 m0, s68, 40960\n"
    "s_nop 0\n"
    "buffer_load_dwordx4 v21, s[52:55], s71 offen offset:0 sc1 lds\n"
    "buffer_load_dwordx4 v21, s[52:55], s71 offen offset:1024 sc1 lds\n"
    "s_and_b32 s63, s58, 15\n"
    "buffer_load_dwordx4 v21, s[52:55], s71 offen offset:2048 sc1 lds\n"
    "s_waitcnt vmcnt(12)\n"
    "s_barrier\n"
    "buffer_load_dwordx4 v21, s[52:55], s71 offen offset:3072 sc1 lds\n"
    "s_lshl_b32 s73, s63, 7\n"
    "s_add_u32 m0, s68, 45056\n"
    "s_mul_i32 s63, s63, 20480\n"
    "buffer_load_dwordx4 v21, s[52:55], s72 offen offset:0 sc1 lds\n"
    "s_add_u32 m0, s67, 0\n"
    "s_add_u32 s74, s63, s69\n"
    "buffer_load_dwordx4 v17, s[48:51], s70 offen offset:0 lds\n"
    "s_lshr_b64 s[58:59], s[58:59], 4\n"
    "buffer_load_dwordx4 v18, s[48:51], s70 offen offset:1024 lds\n"
    "buffer_load_dwordx4 v19, s[48:51], s70 offen offset:2048 lds\n"
    "s_add_u32 s75, s74, 4096\n"
    "buffer_load_dwordx4 v20, s[48:51], s70 offen offset:3072 lds\n"
    "s_add_u32 m0, s68, 61440\n"
    "s_nop 0\n"
    "buffer_load_dwordx4 v21, s[52:55], s74 offen offset:0 sc1 lds\n"
    "buffer_load_dwordx4 v21, s[52:55], s74 offen offset:1024 sc1 lds\n"
    "buffer_load_dwordx4 v21, s[52:55], s74 offen offset:2048 sc1 lds\n"
    "s_and_b32 s63, s58, 15\n"
    "buffer_load_dwordx4 v21, s[52:55], s74 offen offset:3072 sc1 lds\n"
    "s_add_u32 m0, s68, 65536\n"
    "s_waitcnt vmcnt(13)\n"
    "s_barrier\n"
    "buffer_load_dwordx4 v21, s[52:55], s75 offen offset:0 sc1 lds\n"
    "s_lshl_b32 s70, s63, 7\n"
    "s_add_u32 m0, s67, 49152\n"
    "s_mul_i32 s63, s63, 20480\n"
    "buffer_load_dwordx4 v17, s[48:51], s73 offen offset:0 lds\n"
    "buffer_load_dwordx4 v18, s[48:51], s73 offen offset:1024 lds\n"
    "s_add_u32 s71, s63, s69\n"
    "buffer_load_dwordx4 v19, s[48:51], s73 offen offset:2048 lds\n"
    "buffer_load_dwordx4 v20, s[48:51], s73 offen offset:3072 lds\n"
    "s_lshr_b64 s[58:59], s[58:59], 4\n"
    "s_add_u32 s72, s71, 4096\n"
    "s_add_u32 m0, s68, 0\n"
    "s_nop 0\n"
    "buffer_load_dwordx4 v21, s[52:55], s71 offen offset:0 sc1 lds\n"
    "buffer_load_dwordx4 v21, s[52:55], s71 offen offset:1024 sc1 lds\n"
    "s_and_b32 s63, s58, 15\n"
    "buffer_load_dwordx4 v21, s[52:55], s71 offen offset:2048 sc1 lds\n"
    "buffer_load_dwordx4 v21, s[52:55], s71 offen offset:3072 sc1 lds\n"
    "s_lshl_b32 s73, s63, 7\n"
    "s_add_u32 m0, s68, 4096\n"
    "s_mul_i32 s63, s63, 20480\n"
    "buffer_load_dwordx4 v21, s[52:55], s72 offen offset:0 sc1 lds\n"
    "s_add_u32 m0, s67, 16384\n"
    "s_cbranch_vccz .Lc60%=\n"
    "s_cmp_lg_u32 s36, 192\n"
    "s_cbranch_scc1 .Lc61%=\n"
    "s_waitcnt vmcnt(40)\n"
    "v_xor_b32 v41, v32, v36\n"
    "v_xor_b32 v42, v33, v37\n"
    "v_xor_b32 v43, v34, v38\n"
    "v_or3_b32 v41, v41, v42, v43\n"
    "v_xor_b32 v42, v35, v39\n"
    "v_or_b32 v41, v41, v42\n"
    "v_cmp_ne_u32 s[82:83], 0, v41\n"
    "s_nop 0\n"
    "s_cmp_eq_u64 s[82:83], 0\n"
    "s_cbranch_scc1 .Le62%=\n"
    "v_mov_b64 v[36:37], v[32:33]\n"
    "v_mov_b64 v[38:39], v[34:35]\n"
    "buffer_store_dwordx4 v[36:39], v24, s[52:55], s86 offen sc1\n"
    "s_nop 1\n"
    ".Le62%=:\n"
    "buffer_load_dwordx4 v[32:35], v24, s[52:55], s85 offen sc1\n"
    ".Lc61%=:\n"
    ".Lc60%=:\n"
    "s_cbranch_vccnz .Lp63%=\n"
    "s_waitcnt vmcnt(14)\n"
    ".Lp63%=:\n"
    "s_cmp_eq_u32 s36, 192\n"
    "s_cbranch_scc1 .Lq63%=\n"
    "s_waitcnt vmcnt(14)\n"
    ".Lq63%=:\n"
    "s_waitcnt vmcnt(15)\n"
    "s_barrier\n"
    "buffer_load_dwordx4 v17, s[48:51], s70 offen offset:0 lds\n"
    "s_add_u32 s74, s63, s69\n"
    "buffer_load_dwordx4 v18, s[48:51], s70 offen offset:1024 lds\n"
    "buffer_load_dwordx4 v19, s[48:51], s70 offen offset:2048 lds\n"
    "s_lshr_b64 s[58:59], s[58:59], 4\n"
    "buffer_load_dwordx4 v20, s[48:51], s70 offen offset:3072 lds\n"
    "s_add_u32 s75, s74, 4096\n"
    "s_add_u32 m0, s68, 20480\n"
    "s_nop 0\n"
    "buffer_load_dwordx4 v21, s[52:55], s74 offen offset:0 sc1 lds\n"
    "buffer_load_dwordx4 v21, s[52:55], s74 offen offset:1024 sc1 lds\n"
    "buffer_load_dwordx4 v21, s[52:55], s74 offen offset:2048 sc1 lds\n"
    "s_and_b32 s63, s58, 15\n"
    "buffer_load_dwordx4 v21, s[52:55], s74 offen offset:3072 sc1 lds\n"
    "s_add_u32 m0, s68, 24576\n"
    "s_lshl_b32 s70, s63, 7\n"
    "buffer_load_dwordx4 v21, s[52:55], s75 offen offset:0 sc1 lds\n"
    "s_mul_i32 s63, s63, 20480\n"
    "s_add_u32 m0, s67, 32768\n"
    "s_add_u32 s71, s63, s69\n"
    "buffer_load_dwordx4 v17, s[48:51], s73 offen offset:0 lds\n"
    "s_cbranch_vccnz .Lp64%=\n"
    "s_waitcnt vmcnt(15)\n"
    ".Lp64%=:\n"
    "s_cmp_eq_u32 s36, 192\n"
    "s_cbranch_scc1 .Lq64%=\n"
    "s_waitcnt vmcnt(15)\n"
    ".Lq64%=:\n"
    "s_waitcnt vmcnt(16)\n"
    "s_barrier\n"
    "buffer_load_dwordx4 v18, s[48:51], s73 offen offset:1024 lds\n"
    "s_lshr_b64 s[58:59], s[58:59], 4\n"
    "buffer_load_dwordx4 v19, s[48:51], s73 offen offset:2048 lds\n"
    "buffer_load_dwordx4 v20, s[48:51], s73 offen offset:3072 lds\n"
    "s_add_u32 s72, s71, 4096\n"
    "s_add_u32 m0, s68, 40960\n"
    "s_nop 0\n"
    "buffer_load_dwordx4 v21, s[52:55], s71 offen offset:0 sc1 lds\n"
    "buffer_load_dwordx4 v21, s[52:55], s71 offen offset:1024 sc1 lds\n"
    "s_and_b32 s63, s58, 15\n"
    "buffer_load_dwordx4 v21, s[52:55], s71 offen offset:2048 sc1 lds\n"
    "buffer_load_dwordx4 v21, s[52:55], s71 offen offset:3072 sc1 lds\n"
    "s_lshl_b32 s73, s63, 7\n"
    "s_add_u32 m0, s68, 45056\n"
    "s_mul_i32 s63, s63, 20480\n"
    "buffer_load_dwordx4 v21, s[52:55], s72 offen offset:0 sc1 lds\n"
    "s_add_u32 m0, s67, 0\n"
    "s_add_u32 s74, s63, s69\n"
    "buffer_load_dwordx4 v17, s[48:51], s70 offen offset:0 lds\n"
    "s_lshr_b64 s[58:59], s[58:59], 4\n"
    "buffer_load_dwordx4 v18, s[48:51], s70 offen offset:1024 lds\n"
    "s_waitcnt vmcnt(16)\n"
    "s_barrier\n"
    "buffer_load_dwordx4 v19, s[48:51], s70 offen offset:2048 lds\n"
    "s_add_u32 s75, s74, 4096\n"
    "buffer_load_dwordx4 v20, s[48:51], s70 offen offset:3072 lds\n"
    "s_add_u32 m0, s68, 61440\n"
    "s_nop 0\n"
    "buffer_load_dwordx4 v21, s[52:55], s74 offen offset:0 sc1 lds\n"
    "buffer_load_dwordx4 v21, s[52:55], s74 offen offset:1024 sc1 lds\n"
    "buffer_load_dwordx4 v21, s[52:55], s74 offen offset:2048 sc1 lds\n"
    "buffer_load_dwordx4 v21, s[52:55], s74 offen offset:3072 sc1 lds\n"
    "s_add_u32 m0, s68, 65536\n"
    "s_nop 0\n"
    "buffer_load_dwordx4 v21, s[52:55], s75 offen offset:0 sc1 lds\n"
    "s_add_u32 m0, s67, 49152\n"
    "s_nop 0\n"
    "buffer_load_dwordx4 v17, s[48:51], s73 offen offset:0 lds\n"
    "buffer_load_dwordx4 v18, s[48:51], s73 offen offset:1024 lds\n"
    "buffer_load_dwordx4 v19, s[48:51], s73 offen offset:2048 lds\n"
    "buffer_load_dwordx4 v20, s[48:51], s73 offen offset:3072 lds\n"
    "s_waitcnt vmcnt(18)\n"
    "s_barrier\n"
    "s_waitcnt vmcnt(9)\n"
    "s_barrier\n"
    "s_waitcnt vmcnt(0)\n"
    "s_barrier\n"
    "s_cbranch_vccz .Lc65%=\n"
    "s_cmp_lg_u32 s36, 192\n"
    "s_cbranch_scc1 .Lc66%=\n"
    "v_lshlrev_b32 v5, 6, v1\n"
    "s_mov_b32 s81, 65536\n"
    ".Lduty%=:\n"
    "s_mov_b32 s63, 13107200\n"
    "buffer_load_dword v6, v5, s[52:55], s63 offen sc1\n"
    "s_mov_b32 s63, 13111296\n"
    "buffer_load_dword v7, v5, s[52:55], s63 offen sc1\n"
    "s_mov_b32 s63, 13115392\n"
    "buffer_load_dword v8, v5, s[52:55], s63 offen sc1\n"
    "s_mov_b32 s63, 13119488\n"
    "buffer_load_dword v9, v5, s[52:55], s63 offen sc1\n"
    "s_waitcnt vmcnt(0)\n"
    "v_and_b32 v6, v6, v7\n"
    "v_and_b32 v8, v8, v9\n"
    "v_and_b32 v6, v6, v8\n"
    "v_cmp_ne_u32 s[82:83], 1, v6\n"
    "s_nop 0\n"
    "s_cmp_eq_u64 s[82:83], 0\n"
    "s_cbranch_scc1 .Ldone%=\n"
    "s_sub_u32 s81, s81, 1\n"
    "s_cbranch_scc1 .Ldone%=\n"
    "s_cmp_lg_u32 s36, 192\n"
    "s_cbranch_scc1 .Lc67%=\n"
    "buffer_load_dwordx4 v[44:47], v24, s[52:55], s85 offen sc1\n"
    "s_waitcnt vmcnt(0)\n"
    "buffer_store_dwordx4 v[44:47], v24, s[52:55], s86 offen sc1\n"
    ".Lc67%=:\n"
    "s_branch .Lduty%=\n"
    ".Ldone%=:\n"
    ".Lc66%=:\n"
    ".Lc65%=:\n"
      ::"s"(__builtin_amdgcn_kernarg_segment_ptr()), "s"(blockIdx.x), "s"(w), "v"(threadIdx.x)
      : "memory", "scc", "vcc",
"s36", "s37", "s38", "s39", "s40", "s41", "s42", "s43", "s44", "s45", "s46", "s47", "s48", "s49", "s50", "s51", "s52", "s53", "s54", "s55", "s56", "s57", "s58", "s59", "s60", "s61", "s62", "s63", "s64", "s65", "s66", "s67", "s68", "s69", "s70", "s71", "s72", "s73", "s74", "s75", "s76", "s77", "s78", "s79", "s80", "s81", "s82", "s83", "s84", "s85", "s86", "s87", "s88", "s89", "s90", "s91", "s92", "s93", "s94", "s95", "s96", "s97", "v1", "v2", "v3", "v4", "v5", "v6", "v7", "v8", "v9", "v10", "v11", "v12", "v13", "v14", "v15", "v16", "v17", "v18", "v19", "v20", "v21", "v22", "v23", "v24", "v25", "v26", "v27", "v28", "v29", "v30", "v31", "v32", "v33", "v34", "v35", "v36", "v37", "v38", "v39", "v40", "v41", "v42", "v43", "v44", "v45", "v46", "v47");
}

extern "C" void kernel_launch(void* const* d_in, const int* in_sizes, int n_in, void* d_out, int out_size, void* d_ws, size_t ws_size, hipStream_t stream) {
  (void)hipFuncSetAttribute((const void*)fnet_fused, hipFuncAttributeMaxDynamicSharedMemorySize, LDS_BYTES);
  fnet_fused<<<256, 512, LDS_BYTES, stream>>>((const char*)d_in[0], (const char*)d_in[1], (const char*)d_in[2], (const char*)d_in[3], (const char*)d_in[4],
                                              (char*)d_ws, (char*)d_out);
}
```
